```python
import torch

def _softmax(x, axis=-1):
    return torch.softmax(x, dim=axis)
def _gelu(x):
    return torch.nn.functional.gelu(x, approximate="tanh")
def _silu(x):
    return torch.nn.functional.silu(x)
def _sigmoid(x):
    return torch.sigmoid(x)
def _layernorm(x, axis=-1, eps=1e-5):
    m = x.mean(dim=axis, keepdim=True)
    v = x.var(dim=axis, keepdim=True, unbiased=False)
    return (x - m) / torch.sqrt(v + eps)
def _scatter_add(tgt, idx, src, axis=0):
    return tgt.clone().index_add_(axis, idx, src)
def _segment_sum(src, seg, n):
    out = torch.zeros((n,) + src.shape[1:], dtype=src.dtype)
    return out.index_add_(0, seg, src)
def _pad(x, pw):
    flat = tuple(v for pair in reversed(pw) for v in pair)
    return torch.nn.functional.pad(x, flat)

kernel_name = 'sequential_preLN__compressed_kv_cla__dense_activation_gelu__prompt'

def setup_inputs():

    _w = (lambda a: (lambda t: t.to(torch.bfloat16) if t.is_floating_point() else t)(torch.as_tensor(a)))
    def _A_setup():
        B, H, Sq, Sk, D, M = 1, 32, 2048, 2048, 128, 640
        Dc = 4 * D
        return {
            "x": _w((lambda *s: torch.randn(*s, dtype=torch.bfloat16) * 0.05)(B, Sq, M)),
            "c_kv": _w((lambda *s: torch.randn(*s, dtype=torch.bfloat16) * 0.4)(B, Sk, Dc)),
            "w_q": _w((lambda *s: torch.randn(*s, dtype=torch.bfloat16) * 1.6)(M, H, D) * (M ** -0.5)),
            "w_uk": _w((lambda *s: torch.randn(*s, dtype=torch.bfloat16) * 1.6)(Dc, H, D) * (Dc ** -0.5)),
            "w_uv": _w((lambda *s: torch.randn(*s, dtype=torch.bfloat16) * 0.4)(Dc, H, D) * (Dc ** -0.5)),
            "w_o": _w((lambda *s: torch.randn(*s, dtype=torch.bfloat16) * 0.4)(H, D, M) * ((H * D) ** -0.5)),
        }
    _A_in = _A_setup()

    def _B_setup():
        B, S, D, F = 1, 2048, 640, 1728
        return {
            "x": _w((lambda *s: torch.randn(*s, dtype=torch.bfloat16) * 0.05)(B, S, D)),
            "w1": _w((lambda *s: torch.randn(*s, dtype=torch.bfloat16) * 0.8)(D, F) * (D ** -0.5)),
            "b1": _w((lambda *s: torch.randn(*s, dtype=torch.bfloat16) * 12.8)(F) * 0.01),
            "w2": _w((lambda *s: torch.randn(*s, dtype=torch.bfloat16) * 0.2)(F, D) * (F ** -0.5)),
            "b2": _w((lambda *s: torch.randn(*s, dtype=torch.bfloat16) * 3.2)(D) * 0.01),
        }
    _B_in = _B_setup()

    return {
        "A_x": _A_in["x"],
        "A_c_kv": _A_in["c_kv"],
        "A_w_q": _A_in["w_q"],
        "A_w_uk": _A_in["w_uk"],
        "A_w_uv": _A_in["w_uv"],
        "A_w_o": _A_in["w_o"],
        "B_x": _B_in["x"],
        "B_w1": _B_in["w1"],
        "B_b1": _B_in["b1"],
        "B_w2": _B_in["w2"],
        "B_b2": _B_in["b2"],
    }

def reference(A_x, A_c_kv, A_w_q, A_w_uk, A_w_uv, A_w_o, B_x, B_w1, B_b1, B_w2, B_b2):
    def _A_ref(x, c_kv, w_q, w_uk, w_uv, w_o):
        B, Sq, M = x.shape[0], x.shape[1], x.shape[2]
        Sk = c_kv.shape[1]
        H, D = w_q.shape[1], w_q.shape[2]
        q = torch.einsum("bqm,mhd->bhqd", x, w_q)
        k = torch.einsum("bkc,chd->bhkd", c_kv, w_uk)
        v = torch.einsum("bkc,chd->bhkd", c_kv, w_uv)
        scores = torch.einsum("bhqd,bhkd->bhqk", q, k) * (D ** -0.5)
        off = Sk - Sq
        qi = torch.arange(Sq)[:, None]
        kj = torch.arange(Sk)[None, :]
        cmask = kj <= (qi + off)
        scores = torch.where(cmask[None, None], scores, -1e9)
        p = _softmax(scores, axis=-1)
        ctx = torch.einsum("bhqk,bhkd->bhqd", p, v)
        return torch.einsum("bhqd,hdm->bqm", ctx, w_o)

    def _B_ref(x, w1, b1, w2, b2):
        h = torch.einsum("bsd,df->bsf", x, w1) + b1
        h = _gelu(h)
        return torch.einsum("bsf,fd->bsd", h, w2) + b2

    x = A_x
    h = x + _A_ref(_layernorm(x, axis=-1), A_c_kv, A_w_q, A_w_uk, A_w_uv, A_w_o)
    h2 = h + _B_ref(_layernorm(h, axis=-1), B_w1, B_b1, B_w2, B_b2)
    return h2
```

```cpp
#include <hip/hip_runtime.h>
#include <hip/hip_bf16.h>
#include <stdint.h>
#include <type_traits>

typedef __bf16 bf16x8 __attribute__((ext_vector_type(8)));
typedef __bf16 bf16x4 __attribute__((ext_vector_type(4)));
typedef __bf16 bf16x2 __attribute__((ext_vector_type(2)));
typedef short s16x4 __attribute__((ext_vector_type(4)));
typedef short s16x8 __attribute__((ext_vector_type(8)));
typedef float f32x16 __attribute__((ext_vector_type(16)));
typedef float f32x4 __attribute__((ext_vector_type(4)));
typedef float f32x2 __attribute__((ext_vector_type(2)));
typedef unsigned u32x4 __attribute__((ext_vector_type(4)));
typedef unsigned u32x2 __attribute__((ext_vector_type(2)));
typedef __amdgpu_buffer_rsrc_t rsrc_t;
#define LAS __attribute__((address_space(3)))
#define DI __device__ __forceinline__
#define SBAR() __builtin_amdgcn_sched_barrier(0)

constexpr int SEQ = 2048, NH = 32, HD = 128, DM = 640, DC = 512, FF = 1728, HDT = NH * HD;

constexpr size_t WS_XNF = 0;
constexpr size_t WS_KVF = WS_XNF + (size_t)SEQ * DM * 2;
constexpr size_t WS_CTX = WS_KVF + (size_t)SEQ * HDT * 4;
constexpr size_t WS_WOF = WS_CTX + (size_t)SEQ * HDT * 2;
constexpr size_t WS_W1F = WS_WOF + (size_t)HDT * DM * 2;
constexpr size_t WS_W2F = WS_W1F + (size_t)DM * FF * 2;
constexpr size_t WS_HF  = WS_W2F + (size_t)DM * FF * 2;
constexpr size_t WS_MID = WS_HF + (size_t)SEQ * DM * 2;
constexpr size_t WS_ST  = WS_MID + (size_t)SEQ * FF * 2;
constexpr size_t WS_CS1 = WS_ST + (size_t)SEQ * 8 * 8;
constexpr size_t WS_END = WS_CS1 + (size_t)FF * 4;

template <int I, int N, class F> DI void static_for(F&& f) {
  if constexpr (I < N) { f(std::integral_constant<int, I>{}); static_for<I + 1, N>(f); }
}
template <int N> DI void wait_vm() { asm volatile("s_waitcnt vmcnt(%0)" ::"i"(N) : "memory"); }
DI void bar() { asm volatile("s_waitcnt lgkmcnt(0)\n\ts_barrier" ::: "memory"); }
DI rsrc_t mk_rsrc(const void* p, size_t bytes) { return __builtin_amdgcn_make_buffer_rsrc((void*)p, (short)0, (int)bytes, 0x00020000); }
DI void dma16(rsrc_t srd, unsigned voff, unsigned soff, unsigned lds_addr) {
  asm volatile("s_mov_b32 m0, %0\n\ts_nop 0\n\tbuffer_load_dwordx4 %1, %2, %3 offen lds" ::"s"(lds_addr), "v"(voff), "s"(srd), "s"(soff) : "memory");
}
DI void dma16x4(rsrc_t srd, unsigned voff, unsigned soff, unsigned lds_addr) {
  asm volatile("s_mov_b32 m0, %0\n\ts_nop 0\n\t"
               "buffer_load_dwordx4 %1, %2, %3 offen lds\n\t"
               "buffer_load_dwordx4 %1, %2, %3 offen offset:1024 lds\n\t"
               "buffer_load_dwordx4 %1, %2, %3 offen offset:2048 lds\n\t"
               "buffer_load_dwordx4 %1, %2, %3 offen offset:3072 lds" ::"s"(lds_addr), "v"(voff), "s"(srd), "s"(soff) : "memory");
}
template <int IMM> DI void gld16(bf16x8& dst, rsrc_t srd, unsigned voff, unsigned soff) {
  asm volatile("buffer_load_dwordx4 %0, %1, %2, %3 offen offset:%4" : "=v"(dst) : "v"(voff), "s"(srd), "s"(soff), "i"(IMM) : "memory");
}
DI unsigned cvtpk(float lo, float hi) { f32x2 v = {lo, hi}; bf16x2 b = __builtin_convertvector(v, bf16x2); return __builtin_bit_cast(unsigned, b); }
DI bf16x8 lds_rd16(LAS const char* p) { return *(LAS const bf16x8*)p; }
DI s16x4 lds_tr(LAS const char* p) { return __builtin_amdgcn_ds_read_tr16_b64_v4i16((LAS s16x4*)p); }
DI bf16x8 lds_tr2(LAS const char* p0, LAS const char* p1) {
  s16x4 a = lds_tr(p0), b = lds_tr(p1);
  s16x8 c = __builtin_shufflevector(a, b, 0, 1, 2, 3, 4, 5, 6, 7);
  return __builtin_bit_cast(bf16x8, c);
}
DI float bf_lo(unsigned u) { return __uint_as_float(u << 16); }
DI float bf_hi(unsigned u) { return __uint_as_float(u & 0xffff0000u); }
#define MFMA32(a, b, c) __builtin_amdgcn_mfma_f32_32x32x16_bf16(a, b, c, 0, 0, 0)
#define MFMA16(a, b, c) __builtin_amdgcn_mfma_f32_16x16x32_bf16(a, b, c, 0, 0, 0)
DI u32x4 pack8(const f32x16& x, int s, float sc) {
  u32x4 w;
#pragma unroll
  for (int i = 0; i < 4; ++i) w[i] = cvtpk(x[8 * s + 2 * i] * sc, x[8 * s + 2 * i + 1] * sc);
  return w;
}
DI u32x4 pack8n(const f32x16& x, int s) {
  u32x4 w;
#pragma unroll
  for (int i = 0; i < 4; ++i) w[i] = cvtpk(x[8 * s + 2 * i], x[8 * s + 2 * i + 1]);
  return w;
}
DI float wave_sum(float v) {
#pragma unroll
  for (int o = 32; o >= 1; o >>= 1) v += __shfl_xor(v, o);
  return v;
}
DI float swap32_max(float v) {
  auto r = __builtin_amdgcn_permlane32_swap(__float_as_uint(v), __float_as_uint(v), false, false);
  return fmaxf(__uint_as_float(r[0]), __uint_as_float(r[1]));
}
DI float swap32_sum(float v) {
  auto r = __builtin_amdgcn_permlane32_swap(__float_as_uint(v), __float_as_uint(v), false, false);
  return __uint_as_float(r[0]) + __uint_as_float(r[1]);
}

#define KV_ST_AUX 0

DI void relayout_issue(rsrc_t srd, unsigned ldN  , unsigned k0, unsigned n0, unsigned lds_addr, int lane) {
  const unsigned voff = (((lane >> 5) * 4 + ((lane >> 2) & 3)) * ldN + ((lane >> 4) & 1) * 32 + (lane & 3) * 8) * 2;
#pragma unroll
  for (int p = 0; p < 4; ++p) dma16(srd, voff, ((k0 + 8 * p) * ldN + n0) * 2, lds_addr + p * 1024);
}
template <int PERM> DI void relayout_finish(LAS const char* img, char* dst  , size_t ng_stride, int lane) {
  const int g = lane >> 4, q = (lane >> 2) & 3, p = lane & 3;
  const int rg0 = PERM == 0 ? 4 * (g >> 1) + (g & 1) : g;
  const int rg1 = PERM == 0 ? rg0 + 2 : g + 4;
#pragma unroll
  for (int i = 0; i < 4; ++i) {
    const int o = (i >> 1) * 256 + q * 64 + (i & 1) * 32 + p * 8;
    bf16x8 f = lds_tr2(img + rg0 * 512 + o, img + rg1 * 512 + o);
    *(bf16x8*)(dst + i * ng_stride + lane * 16) = f;
  }
}

constexpr int K1_SLOT = 49152, K1_LDS = 3 * K1_SLOT;
constexpr int k1_n(int s) { return s < 8 ? 6 : (s < 12 ? 4 : 0); }
constexpr int k1_c(int s) { return s < 7 ? 0 : (s == 7 ? 8 : 2); }

template <bool IS_V>
DI void kv_body(LAS char* lds, rsrc_t sC, rsrc_t sWk, rsrc_t sWv, rsrc_t sKV, int h, int kb, int wid, int lane) {
  const unsigned ldsb = (unsigned)(size_t)lds;
  const int l31 = lane & 31, hh = lane >> 5, db = wid & 3;
  const unsigned vC = (unsigned)(((lane >> 3) * DC + (((lane & 7) ^ ((4 * (wid & 1) + (lane >> 4)) & 7)) * 8)) * 2);
  const unsigned vW = (unsigned)((((lane >> 2) & 3) * HDT + (lane >> 4) * 32 + (lane & 3) * 8) * 2);
  const unsigned key0 = kb * 256;
  auto issue = [&](auto S_) {
    constexpr int s = decltype(S_)::value;
    constexpr unsigned slot = (s % 3) * K1_SLOT;
    if constexpr (s < 8) {
#pragma unroll
      for (int i = 0; i < 2; ++i) {
        const unsigned p = wid + 8 * i;
        dma16(sC, vC, ((key0 + 8 * p) * DC + s * 64) * 2, ldsb + slot + p * 1024);
        dma16(sWk, vW, ((s * 64 + 4 * p) * HDT + h * HD) * 2, ldsb + slot + 16384 + p * 1024);
        dma16(sWv, vW, ((s * 64 + 4 * p) * HDT + h * HD) * 2, ldsb + slot + 32768 + p * 1024);
      }
    } else if constexpr (s < 12) {
#pragma unroll
      for (int i = 0; i < 4; ++i) {
        const unsigned pr = wid & 3, cs = (wid >> 2) + 2 * i;
        dma16(sC, vC, ((key0 + 128 + 32 * (s - 8) + 8 * pr) * DC + cs * 64) * 2, ldsb + slot + cs * 4096 + pr * 1024);
      }
    }
  };
  issue(std::integral_constant<int, 0>{});
  issue(std::integral_constant<int, 1>{});
  LAS const char* cb[4];
#pragma unroll
  for (int k = 0; k < 4; ++k) cb[k] = lds + l31 * 128 + (((2 * k + hh) ^ ((lane >> 1) & 7)) * 16);
  LAS const char* wb = lds + 16384 + (IS_V ? 16384 : 0) + hh * 2048 + ((lane >> 2) & 3) * 64 + ((lane >> 4) & 1) * 32 + (lane & 3) * 8 + db * 256;
  const unsigned vout = lane * 16 + db * 2048 + (IS_V ? 16384 : 0);
  bf16x8 wf[32];
  f32x16 acc[4];
#pragma unroll
  for (int i = 0; i < 4; ++i) acc[i] = f32x16{};
  auto store_tile = [&](const f32x16& a, int kt  ) {
    const unsigned gk = kb * 8 + kt;
    const unsigned so = (h * 32 + (gk >> 1)) * 32768 + (gk & 1) * 8192;
#pragma unroll
    for (int s = 0; s < 2; ++s) __builtin_amdgcn_raw_buffer_store_b128(pack8n(a, s), sKV, vout + s * 1024, so, KV_ST_AUX);
  };
  static_for<0, 12>([&](auto S_) {
    constexpr int s = decltype(S_)::value;
    constexpr unsigned slot = (s % 3) * K1_SLOT;
    constexpr int N = (s >= 2 ? k1_c(s - 2) : 0) + k1_n(s + 1) + (s >= 1 ? k1_c(s - 1) : 0);
    wait_vm<N>();
    bar();
    issue(std::integral_constant<int, s + 2>{});
    if constexpr (s < 8) {
#pragma unroll
      for (int k = 0; k < 4; ++k) wf[4 * s + k] = lds_tr2(wb + slot + k * 4096, wb + slot + k * 4096 + 1024);
#pragma unroll
      for (int k = 0; k < 4; ++k)
#pragma unroll
        for (int b = 0; b < 4; ++b) {
          bf16x8 c = lds_rd16(cb[k] + slot + b * 4096);
          if constexpr (IS_V) acc[b] = MFMA32(c, wf[4 * s + k], acc[b]); else acc[b] = MFMA32(wf[4 * s + k], c, acc[b]);
        }
      if constexpr (s == 7) {
#pragma unroll
        for (int b = 0; b < 4; ++b) store_tile(acc[b], b);
      }
    } else {
      f32x16 a = f32x16{};
#pragma unroll
      for (int k = 0; k < 32; ++k) {
        bf16x8 c = lds_rd16(cb[k & 3] + slot + (k >> 2) * 4096);
        if constexpr (IS_V) a = MFMA32(c, wf[k], a); else a = MFMA32(wf[k], c, a);
      }
      store_tile(a, 4 + (s - 8));
    }
  });
}

__global__ __launch_bounds__(512, 2) void k1_kvproj(const __bf16* __restrict__ x, const __bf16* __restrict__ ckv, const __bf16* __restrict__ wuk,
                                                    const __bf16* __restrict__ wuv, const __bf16* __restrict__ w1, char* __restrict__ ws) {
  extern __shared__ __attribute__((aligned(16))) char smem[];
  LAS char* lds = (LAS char*)smem;
  const int bid = blockIdx.x, tid = threadIdx.x, lane = tid & 63;
  const int wid = __builtin_amdgcn_readfirstlane(tid >> 6);
  const int idx = bid >> 3, h = (bid & 7) + 8 * (idx & 3), kb = idx >> 2;
  rsrc_t sC = mk_rsrc(ckv, (size_t)SEQ * DC * 2), sWk = mk_rsrc(wuk, (size_t)DC * HDT * 2), sWv = mk_rsrc(wuv, (size_t)DC * HDT * 2);
  rsrc_t sKV = mk_rsrc(ws + WS_KVF, (size_t)SEQ * HDT * 4);
  if (wid < 4) kv_body<false>(lds, sC, sWk, sWv, sKV, h, kb, wid, lane);
  else kv_body<true>(lds, sC, sWk, sWv, sKV, h, kb, wid, lane);

  {
    const int r = bid * 8 + wid;
    const u32x4 a = *(const u32x4*)(x + (size_t)r * DM + lane * 8);
    u32x4 b = {0, 0, 0, 0};
    if (lane < 16) b = *(const u32x4*)(x + (size_t)r * DM + 512 + lane * 8);
    float v[16];
#pragma unroll
    for (int i = 0; i < 4; ++i) { v[2 * i] = bf_lo(a[i]); v[2 * i + 1] = bf_hi(a[i]); v[8 + 2 * i] = bf_lo(b[i]); v[9 + 2 * i] = bf_hi(b[i]); }
    float s = 0;
#pragma unroll
    for (int i = 0; i < 16; ++i) s += v[i];
    const float mean = wave_sum(s) * (1.0f / DM);
    float q = 0;
#pragma unroll
    for (int i = 0; i < 8; ++i) { const float d = v[i] - mean; q += d * d; }
    if (lane < 16) {
#pragma unroll
      for (int i = 8; i < 16; ++i) { const float d = v[i] - mean; q += d * d; }
    }
    const float rstd = 1.0f / sqrtf(wave_sum(q) * (1.0f / DM) + 1e-5f);
    char* xnf = ws + WS_XNF + (size_t)(r >> 5) * 40 * 1024 + (r & 31) * 16;
    u32x4 w;
#pragma unroll
    for (int i = 0; i < 4; ++i) w[i] = cvtpk((v[2 * i] - mean) * rstd, (v[2 * i + 1] - mean) * rstd);
    *(u32x4*)(xnf + (lane >> 1) * 1024 + (lane & 1) * 512) = w;
    if (lane < 16) {
#pragma unroll
      for (int i = 0; i < 4; ++i) w[i] = cvtpk((v[8 + 2 * i] - mean) * rstd, (v[9 + 2 * i] - mean) * rstd);
      *(u32x4*)(xnf + (32 + (lane >> 1)) * 1024 + (lane & 1) * 512) = w;
    }
  }
  if (bid < FF / 8) {
    const u32x4 a = *(const u32x4*)(w1 + (size_t)tid * FF + bid * 8);
    u32x4 b = {0, 0, 0, 0};
    if (tid < DM - 512) b = *(const u32x4*)(w1 + (size_t)(tid + 512) * FF + bid * 8);
    float v[8];
#pragma unroll
    for (int i = 0; i < 4; ++i) { v[2 * i] = bf_lo(a[i]) + bf_lo(b[i]); v[2 * i + 1] = bf_hi(a[i]) + bf_hi(b[i]); }
#pragma unroll
    for (int i = 0; i < 8; ++i) v[i] = wave_sum(v[i]);
    bar();
    LAS float* red = (LAS float*)lds;
    if (lane == 0) {
#pragma unroll
      for (int i = 0; i < 8; ++i) red[wid * 8 + i] = v[i];
    }
    bar();
    if (tid < 8) {
      float s = 0;
#pragma unroll
      for (int w = 0; w < 8; ++w) s += red[w * 8 + tid];
      ((float*)(ws + WS_CS1))[bid * 8 + tid] = s;
    }
  }
}

constexpr int K2_QB = 131072, K2_LDS = 163840;
constexpr float QSCALE = 0.08838834764831845f * 1.4426950408889634f;
constexpr float THR = 8.0f;

struct AttnState { f32x16 o[4]; f32x16 negm; float m, l; };

DI void attn_step(LAS const char* kb  , const bf16x8 (&q)[8], AttnState& st, bool first, bool mask, int dq  ) {
  f32x16 s0, s1;
#pragma unroll
  for (int k = 0; k < 8; ++k) {
    bf16x8 a0 = lds_rd16(kb + k * 1024), a1 = lds_rd16(kb + 8192 + k * 1024);
    if (k == 0) { s0 = MFMA32(a0, q[0], st.negm); s1 = MFMA32(a1, q[0], st.negm); }
    else { s0 = MFMA32(a0, q[k], s0); s1 = MFMA32(a1, q[k], s1); }
  }
  if (mask) {
    const float NEG = -__builtin_inff();
#pragma unroll
    for (int r = 0; r < 16; ++r) {
      const int c = (r & 3) + 8 * (r >> 2);
      if (c > dq) s0[r] = NEG;
      if (c + 32 > dq) s1[r] = NEG;
    }
  }
  float ma = fmaxf(fmaxf(s0[0], s0[1]), s1[0]), mb = fmaxf(fmaxf(s0[2], s0[3]), s1[1]);
  ma = fmaxf(fmaxf(ma, s1[2]), s1[3]);
#pragma unroll
  for (int r = 4; r < 16; r += 4) {
    ma = fmaxf(fmaxf(ma, s0[r]), s0[r + 1]); mb = fmaxf(fmaxf(mb, s0[r + 2]), s0[r + 3]);
    ma = fmaxf(fmaxf(ma, s1[r]), s1[r + 1]); mb = fmaxf(fmaxf(mb, s1[r + 2]), s1[r + 3]);
  }
  const float rmax = swap32_max(fmaxf(ma, mb));
  if (__builtin_expect(first || __any(rmax > THR), 0)) {
    const float dl = first ? rmax : fmaxf(rmax, 0.f);
    const float f = __builtin_amdgcn_exp2f(-fmaxf(dl, 0.f));
    st.m += dl; st.l *= f;
#pragma unroll
    for (int r = 0; r < 16; ++r) { s0[r] -= dl; s1[r] -= dl; st.negm[r] = -st.m; }
#pragma unroll
    for (int d = 0; d < 4; ++d)
#pragma unroll
      for (int r = 0; r < 16; ++r) st.o[d][r] *= f;
  }
  float sa = 0.f, sb = 0.f;
#pragma unroll
  for (int r = 0; r < 16; ++r) { s0[r] = __builtin_amdgcn_exp2f(s0[r]); sa += s0[r]; }
#pragma unroll
  for (int r = 0; r < 16; ++r) { s1[r] = __builtin_amdgcn_exp2f(s1[r]); sb += s1[r]; }
  st.l += sa + sb;
  bf16x8 p[4];
  p[0] = __builtin_bit_cast(bf16x8, pack8n(s0, 0)); p[1] = __builtin_bit_cast(bf16x8, pack8n(s0, 1));
  p[2] = __builtin_bit_cast(bf16x8, pack8n(s1, 0)); p[3] = __builtin_bit_cast(bf16x8, pack8n(s1, 1));
#pragma unroll
  for (int i = 0; i < 4; ++i)
#pragma unroll
    for (int d = 0; d < 4; ++d) {
      bf16x8 v = lds_rd16(kb + 16384 + (i >> 1) * 8192 + d * 2048 + (i & 1) * 1024);
      st.o[d] = MFMA32(v, p[i], st.o[d]);
    }
}

DI void attn_reset(AttnState& st) {
#pragma unroll
  for (int d = 0; d < 4; ++d) st.o[d] = f32x16{};
  st.negm = f32x16{}; st.m = 0.f; st.l = 0.f;
}

__global__ __launch_bounds__(512, 2) void k2_attn(const __bf16* __restrict__ wq, const __bf16* __restrict__ wo, char* __restrict__ ws) {
  extern __shared__ __attribute__((aligned(16))) char smem[];
  LAS char* lds = (LAS char*)smem;
  const unsigned ldsb = (unsigned)(size_t)lds;
  const int bid = blockIdx.x, tid = threadIdx.x, lane = tid & 63;
  const int wid = __builtin_amdgcn_readfirstlane(tid >> 6);
  const int idx = bid >> 3, h = (bid & 7) + 8 * (idx & 3), j = idx >> 2;
  const int grp = wid >> 2, rg = wid & 3, l31 = lane & 31, hh = lane >> 5;
  const int blkB = 15 - j;
  rsrc_t sWq = mk_rsrc(wq, (size_t)DM * HDT * 2), sWo = mk_rsrc(wo, (size_t)HDT * DM * 2);
  rsrc_t sX = mk_rsrc(ws + WS_XNF, (size_t)SEQ * DM * 2), sKV = mk_rsrc(ws + WS_KVF, (size_t)SEQ * HDT * 4);
  rsrc_t sCtx = mk_rsrc(ws + WS_CTX, (size_t)SEQ * HDT * 2);
  const unsigned vl16 = lane * 16;
  const unsigned vW = (unsigned)((((lane >> 2) & 3) * HDT + (lane >> 4) * 32 + (lane & 3) * 8) * 2);
  auto chunk_off = [](int c) { return c == 0 ? (unsigned)K2_QB : (unsigned)(c - 1) * 32768u; };
  auto issueW = [&](int c) {
#pragma unroll
    for (int i = 0; i < 4; ++i) { const unsigned p = wid * 4 + i; dma16(sWq, vW, ((c * 128 + 4 * p) * HDT + h * HD) * 2, ldsb + chunk_off(c) + p * 1024); }
  };
  const unsigned xbase = (unsigned)(((grp ? blkB : j) * 4 + rg) * 40) * 1024u;
  bf16x8 xf[3][8];
  auto issueX = [&](bf16x8(&d)[8], int c) {
    const unsigned so = xbase + c * 8192;
    gld16<0>(d[0], sX, vl16, so); gld16<1024>(d[1], sX, vl16, so); gld16<2048>(d[2], sX, vl16, so); gld16<3072>(d[3], sX, vl16, so);
    gld16<0>(d[4], sX, vl16, so + 4096); gld16<1024>(d[5], sX, vl16, so + 4096); gld16<2048>(d[6], sX, vl16, so + 4096); gld16<3072>(d[7], sX, vl16, so + 4096);
  };
#define PINX(d) asm volatile("" : "+v"(d[0]), "+v"(d[1]), "+v"(d[2]), "+v"(d[3]), "+v"(d[4]), "+v"(d[5]), "+v"(d[6]), "+v"(d[7]))
  auto issueKV = [&](int u) {
    const unsigned so = (unsigned)(h * 32 + 2 * u) * 32768u + wid * 8192, la = ldsb + (u & 1) * 65536 + wid * 8192;
    dma16x4(sKV, vl16, so, la); dma16x4(sKV, vl16, so + 4096, la + 4096);
  };
  f32x16 qa[4];
#pragma unroll
  for (int d = 0; d < 4; ++d) qa[d] = f32x16{};
  LAS const char* wb = lds + hh * 2048 + ((lane >> 2) & 3) * 64 + ((lane >> 4) & 1) * 32 + (lane & 3) * 8;
  auto qchunk = [&](const bf16x8(&xs)[8], int c) {
    LAS const char* b = wb + chunk_off(c);
#pragma unroll
    for (int k = 0; k < 8; ++k)
#pragma unroll
      for (int d = 0; d < 4; ++d) {
        bf16x8 w = lds_tr2(b + k * 4096 + d * 256, b + k * 4096 + d * 256 + 1024);
        qa[d] = MFMA32(w, xs[k], qa[d]);
      }
  };
  issueX(xf[0], 0); issueW(0); issueX(xf[1], 1); issueW(1); issueW(2); issueW(3); issueW(4);
  const int un = bid * 8 + wid;
  const int unc = un < 1280 ? un : un - 1280;
  const int uks = unc / 10, unb = unc - uks * 10;
  wait_vm<24>(); PINX(xf[0]); bar();
  issueX(xf[2], 2);
  qchunk(xf[0], 0);
  wait_vm<20>(); PINX(xf[1]); bar();
  relayout_issue(sWo, DM, uks * 32, unb * 64, ldsb + K2_QB + wid * 4096, lane);
  issueX(xf[0], 3);
  qchunk(xf[1], 1);
  wait_vm<12>(); PINX(xf[2]); bar();
  issueX(xf[1], 4);
  qchunk(xf[2], 2);
  wait_vm<8>(); PINX(xf[0]); bar();
  issueKV(0);
  qchunk(xf[0], 3);
  if (un < 1280) relayout_finish<0>(lds + K2_QB + wid * 4096, ws + WS_WOF + ((size_t)(unb * 4) * 128 + uks) * 1024, (size_t)128 * 1024, lane);
  wait_vm<8>(); PINX(xf[1]); bar();
  qchunk(xf[1], 4);
  bf16x8 q[8];
#pragma unroll
  for (int d = 0; d < 4; ++d) { q[2 * d] = __builtin_bit_cast(bf16x8, pack8(qa[d], 0, QSCALE)); q[2 * d + 1] = __builtin_bit_cast(bf16x8, pack8(qa[d], 1, QSCALE)); }
  if (grp) {
#pragma unroll
    for (int k = 0; k < 8; ++k) *(LAS bf16x8*)(lds + K2_QB + rg * 8192 + k * 1024 + lane * 16) = q[k];
  }
  AttnState st; attn_reset(st);
  auto store_ctx = [&](const f32x16& o, int d, float sc, int row0) {
    const unsigned tg = (unsigned)(row0 + l31) >> 4;
    const unsigned vo = (tg * 128 + h * 4 + d) * 1024 + ((l31 & 15) + 16 * hh) * 16;
#pragma unroll
    for (int c = 0; c < 2; ++c) __builtin_amdgcn_raw_buffer_store_b128(pack8(o, c, sc), sCtx, vo + c * 512, 0, 0);
  };
  const int nsup = 16 - j;
  bool first = true;
  int row0 = (grp ? blkB : j) * 128 + rg * 32;
  for (int u = 0; u < nsup; ++u) {
    wait_vm<0>(); bar();
    if (u + 1 < nsup) issueKV(u + 1);
    LAS const char* sl = lds + (u & 1) * 65536 + lane * 16;
    int t0 = 0, t1 = 2;
    if (u > j) { t0 = grp ? 0 : 1; t1 = t0 + 1; }
    for (int t = t0; t < t1; ++t) {
      const int key0 = u * 128 + t * 64;
      if (key0 > row0) break;
      attn_step(sl + t * 32768, q, st, first, key0 + 63 > row0, row0 + l31 - 4 * hh - key0);
      first = false;
    }
    if (u == j && grp == 0) {
      const float lt = swap32_sum(st.l), inv = 1.0f / lt;
#pragma unroll
      for (int d = 0; d < 4; ++d) store_ctx(st.o[d], d, inv, row0);
      attn_reset(st); first = true; row0 = blkB * 128 + rg * 32;
#pragma unroll
      for (int k = 0; k < 8; ++k) q[k] = lds_rd16(lds + K2_QB + rg * 8192 + k * 1024 + lane * 16);
    }
  }
  if (first) st.m = -1e30f;
  bar();
  {
    const float lt = swap32_sum(st.l);
    LAS char* mine = lds + wid * 8704;
    LAS const char* other = lds + (wid ^ 4) * 8704;
    const int dg = grp ? 2 : 0;
#pragma unroll
    for (int d = 0; d < 2; ++d)
#pragma unroll
      for (int r = 0; r < 4; ++r) {
        const f32x16& o = grp ? st.o[2 + d] : st.o[d];
        *(LAS f32x4*)(mine + (d * 4 + r) * 1024 + lane * 16) = (f32x4){o[4 * r], o[4 * r + 1], o[4 * r + 2], o[4 * r + 3]};
      }
    *(LAS f32x2*)(mine + 8192 + lane * 8) = (f32x2){st.m, lt};
    bar();
    const f32x2 ml = *(LAS const f32x2*)(other + 8192 + lane * 8);
    const float mn = fmaxf(st.m, ml[0]);
    const float a = __builtin_amdgcn_exp2f(st.m - mn), b = __builtin_amdgcn_exp2f(ml[0] - mn);
    const float inv = 1.0f / (a * lt + b * ml[1]);
    const float fa = a * inv, fb = b * inv;
#pragma unroll
    for (int d = 0; d < 2; ++d) {
      f32x16 o = grp ? st.o[d] : st.o[2 + d];
#pragma unroll
      for (int r = 0; r < 4; ++r) {
        const f32x4 t = *(LAS const f32x4*)(other + (d * 4 + r) * 1024 + lane * 16);
#pragma unroll
        for (int e = 0; e < 4; ++e) o[4 * r + e] = o[4 * r + e] * fa + t[e] * fb;
      }
      store_ctx(o, (2 - dg) + d, 1.0f, row0);
    }
  }
}

constexpr int K3_LDS = 4 * 20480;
template <int MODE>
__global__ __launch_bounds__(256, 1) void k35_gemm(const __bf16* __restrict__ x, const __bf16* __restrict__ b2, const __bf16* __restrict__ w1,
                                                   const __bf16* __restrict__ w2, __bf16* __restrict__ out, char* __restrict__ ws) {
  extern __shared__ __attribute__((aligned(16))) char smem[];
  LAS char* lds = (LAS char*)smem;
  constexpr int NKS = MODE == 0 ? 128 : 54;
  const int bid = blockIdx.x, tid = threadIdx.x, lane = tid & 63;
  const int wid = __builtin_amdgcn_readfirstlane(tid >> 6);
  const int mb = (bid & 7) * 4 + ((bid >> 3) & 3), nb = bid >> 5;
  const char* Af = ws + (MODE == 0 ? WS_CTX : WS_MID) + (size_t)(mb * 4) * NKS * 1024 + lane * 16;
  const char* Bf = ws + (MODE == 0 ? WS_WOF : WS_W2F) + (size_t)(nb * 5) * NKS * 1024 + lane * 16;
  const int ks0 = (NKS * wid) / 4, ks1 = (NKS * (wid + 1)) / 4;
  f32x4 acc[5][4];
#pragma unroll
  for (int n = 0; n < 5; ++n)
#pragma unroll
    for (int t = 0; t < 4; ++t) acc[n][t] = f32x4{};
  constexpr int PD = 4;
  bf16x8 fa[PD][4], fb[PD][5];
  auto load = [&](bf16x8(&a)[4], bf16x8(&b)[5], int ks) {
    ks = ks < ks1 ? ks : ks1 - 1;
#pragma unroll
    for (int t = 0; t < 4; ++t) a[t] = *(const bf16x8*)(Af + ((size_t)t * NKS + ks) * 1024);
#pragma unroll
    for (int n = 0; n < 5; ++n) b[n] = *(const bf16x8*)(Bf + ((size_t)n * NKS + ks) * 1024);
  };
#pragma unroll
  for (int i = 0; i < PD; ++i) load(fa[i], fb[i], ks0 + i);
  if constexpr (MODE == 0) {
    rsrc_t sW1 = mk_rsrc(w1, (size_t)DM * FF * 2), sW2 = mk_rsrc(w2, (size_t)DM * FF * 2);
    const unsigned la = (unsigned)(size_t)lds + wid * 4096;
    for (int un = bid * 4 + wid; un < 1080; un += 1024) {
      if (un < 540) {
        const int ks = un / 27, nbk = un - ks * 27;
        relayout_issue(sW1, FF, ks * 32, nbk * 64, la, lane);
        wait_vm<0>();
        relayout_finish<1>(lds + wid * 4096, ws + WS_W1F + ((size_t)(nbk * 4) * 20 + ks) * 1024, (size_t)20 * 1024, lane);
      } else {
        const int u2 = un - 540, ks = u2 / 10, nbk = u2 - ks * 10;
        relayout_issue(sW2, DM, ks * 32, nbk * 64, la, lane);
        wait_vm<0>();
        relayout_finish<1>(lds + wid * 4096, ws + WS_W2F + ((size_t)(nbk * 4) * 54 + ks) * 1024, (size_t)54 * 1024, lane);
      }
      asm volatile("s_waitcnt lgkmcnt(0)" ::: "memory");
    }
  }
  for (int ks = ks0; ks < ks1; ks += PD) {
#pragma unroll
    for (int i = 0; i < PD; ++i) {
      if (ks + i < ks1) {
#pragma unroll
        for (int n = 0; n < 5; ++n)
#pragma unroll
          for (int t = 0; t < 4; ++t) acc[n][t] = MFMA16(fb[i][n], fa[i][t], acc[n][t]);
      }
      load(fa[i], fb[i], ks + i + PD);
    }
  }
  bar();
#pragma unroll
  for (int t = 0; t < 4; ++t)
    if (t != wid) {
#pragma unroll
      for (int n = 0; n < 5; ++n) *(LAS f32x4*)(lds + wid * 20480 + (t * 5 + n) * 1024 + lane * 16) = acc[n][t];
    }
  bar();
  f32x4 r[5];
#pragma unroll
  for (int n = 0; n < 5; ++n) r[n] = wid == 0 ? acc[n][0] : (wid == 1 ? acc[n][1] : (wid == 2 ? acc[n][2] : acc[n][3]));
#pragma unroll
  for (int o = 1; o < 4; ++o) {
    const int w2_ = (wid + o) & 3;
#pragma unroll
    for (int n = 0; n < 5; ++n) r[n] += *(LAS const f32x4*)(lds + w2_ * 20480 + (wid * 5 + n) * 1024 + lane * 16);
  }
  const int tg = mb * 4 + wid, tok = tg * 16 + (lane & 15), g = lane >> 4;
  char* hf = ws + WS_HF + (size_t)tg * 20 * 1024 + lane * 16;
  if constexpr (MODE == 0) {
    float v[20]; float s = 0.f;
#pragma unroll
    for (int n = 0; n < 5; ++n) {
      const int ng = nb * 5 + n;
      const u32x2 xv = *(const u32x2*)(x + (size_t)tok * DM + ng * 16 + 4 * g);
      u32x2 w;
      w[0] = cvtpk(r[n][0] + bf_lo(xv[0]), r[n][1] + bf_hi(xv[0]));
      w[1] = cvtpk(r[n][2] + bf_lo(xv[1]), r[n][3] + bf_hi(xv[1]));
      *(u32x2*)(hf + (ng >> 1) * 1024 + (ng & 1) * 8) = w;
      v[4 * n] = bf_lo(w[0]); v[4 * n + 1] = bf_hi(w[0]); v[4 * n + 2] = bf_lo(w[1]); v[4 * n + 3] = bf_hi(w[1]);
      s += (v[4 * n] + v[4 * n + 1]) + (v[4 * n + 2] + v[4 * n + 3]);
    }
    s += __shfl_xor(s, 16); s += __shfl_xor(s, 32);
    const float mean = s * (1.0f / 80.0f);
    float q = 0.f;
#pragma unroll
    for (int i = 0; i < 20; ++i) { const float d = v[i] - mean; q += d * d; }
    q += __shfl_xor(q, 16); q += __shfl_xor(q, 32);
    if (g == 0) *(f32x2*)(ws + WS_ST + ((size_t)tok * 8 + nb) * 8) = (f32x2){mean, q};
  } else {
#pragma unroll
    for (int n = 0; n < 5; ++n) {
      const int ng = nb * 5 + n;
      const u32x2 hv = *(const u32x2*)(hf + (ng >> 1) * 1024 + (ng & 1) * 8);
      const u32x2 bv = *(const u32x2*)(b2 + ng * 16 + 4 * g);
      u32x2 w;
      w[0] = cvtpk(r[n][0] + bf_lo(bv[0]) + bf_lo(hv[0]), r[n][1] + bf_hi(bv[0]) + bf_hi(hv[0]));
      w[1] = cvtpk(r[n][2] + bf_lo(bv[1]) + bf_lo(hv[1]), r[n][3] + bf_hi(bv[1]) + bf_hi(hv[1]));
      *(u32x2*)(out + (size_t)tok * DM + ng * 16 + 4 * g) = w;
    }
  }
}

constexpr int K4_LDS = 140 * 1024;
DI float gelu_tanh(float v) {
  const float u = v * (0.7978845608028654f + 0.035677408136300125f * v * v);
  const float e = __builtin_amdgcn_exp2f(u * -2.885390081777927f);
  return v * __builtin_amdgcn_rcpf(1.0f + e);
}
__global__ __launch_bounds__(256, 1) void k4_ffn1(const __bf16* __restrict__ b1, char* __restrict__ ws) {
  extern __shared__ __attribute__((aligned(16))) char smem[];
  LAS char* lds = (LAS char*)smem;
  const unsigned ldsb = (unsigned)(size_t)lds;
  const int bid = blockIdx.x, tid = threadIdx.x, lane = tid & 63;
  const int wid = __builtin_amdgcn_readfirstlane(tid >> 6);
  const int mb = (bid & 7) * 2 + ((bid >> 3) & 1), nbk = bid >> 4;
  const int ng0 = nbk < 12 ? nbk * 7 : 84 + (nbk - 12) * 6, nng = nbk < 12 ? 7 : 6;
  rsrc_t sW = mk_rsrc(ws + WS_W1F, (size_t)DM * FF * 2), sH = mk_rsrc(ws + WS_HF, (size_t)SEQ * DM * 2);
  const unsigned vl16 = lane * 16;
  const int tg0 = mb * 8 + wid * 2;
  bf16x8 hf[2][20];
  static_for<0, 5>([&](auto C_) {
    constexpr int c = decltype(C_)::value;
#pragma unroll
    for (int t = 0; t < 2; ++t) {
      const unsigned so = (unsigned)((tg0 + t) * 20 + 4 * c) * 1024u;
      gld16<0>(hf[t][4 * c], sH, vl16, so); gld16<1024>(hf[t][4 * c + 1], sH, vl16, so);
      gld16<2048>(hf[t][4 * c + 2], sH, vl16, so); gld16<3072>(hf[t][4 * c + 3], sH, vl16, so);
    }
#pragma unroll
    for (int n = 0; n < 7; ++n) {
      const int nn = n < nng ? n : nng - 1;
      dma16(sW, vl16, (unsigned)((ng0 + nn) * 20 + 4 * c + wid) * 1024u, ldsb + (n * 20 + 4 * c + wid) * 1024);
    }
  });
  f32x4 acc[2][7];
#pragma unroll
  for (int t = 0; t < 2; ++t)
#pragma unroll
    for (int n = 0; n < 7; ++n) acc[t][n] = f32x4{};
  static_for<0, 5>([&](auto C_) {
    constexpr int c = decltype(C_)::value;
    wait_vm<15 * (4 - c)>();
    asm volatile("" : "+v"(hf[0][4 * c]), "+v"(hf[0][4 * c + 1]), "+v"(hf[0][4 * c + 2]), "+v"(hf[0][4 * c + 3]),
                      "+v"(hf[1][4 * c]), "+v"(hf[1][4 * c + 1]), "+v"(hf[1][4 * c + 2]), "+v"(hf[1][4 * c + 3]));
    bar();
#pragma unroll
    for (int k = 0; k < 4; ++k)
#pragma unroll
      for (int n = 0; n < 7; ++n) {
        bf16x8 w = lds_rd16(lds + (n * 20 + 4 * c + k) * 1024 + lane * 16);
        acc[0][n] = MFMA16(w, hf[0][4 * c + k], acc[0][n]);
        acc[1][n] = MFMA16(w, hf[1][4 * c + k], acc[1][n]);
      }
  });
  const int g = lane >> 4;
  const float* cs1 = (const float*)(ws + WS_CS1);
#pragma unroll
  for (int t = 0; t < 2; ++t) {
    const int tok = (tg0 + t) * 16 + (lane & 15);
    const f32x4* sp = (const f32x4*)(ws + WS_ST + (size_t)tok * 64);
    float mean = 0.f, m2 = 0.f; float mu[8];
#pragma unroll
    for (int i = 0; i < 4; ++i) { const f32x4 s = sp[i]; mu[2 * i] = s[0]; mu[2 * i + 1] = s[2]; mean += s[0] + s[2]; m2 += s[1] + s[3]; }
    mean *= 0.125f;
#pragma unroll
    for (int i = 0; i < 8; ++i) { const float d = mu[i] - mean; m2 += 80.0f * d * d; }
    const float rstd = 1.0f / sqrtf(m2 * (1.0f / DM) + 1e-5f);
    const float mr = -mean * rstd;
    char* mid = ws + WS_MID + (size_t)(tg0 + t) * 54 * 1024 + lane * 16;
#pragma unroll
    for (int n = 0; n < 7; ++n) {
      if (n < nng) {
        const int ng = ng0 + n;
        const f32x4 cs = *(const f32x4*)(cs1 + ng * 16 + 4 * g);
        const u32x2 bv = *(const u32x2*)(b1 + ng * 16 + 4 * g);
        const float bb[4] = {bf_lo(bv[0]), bf_hi(bv[0]), bf_lo(bv[1]), bf_hi(bv[1])};
        float y[4];
#pragma unroll
        for (int e = 0; e < 4; ++e) y[e] = gelu_tanh(acc[t][n][e] * rstd + (mr * cs[e] + bb[e]));
        u32x2 w; w[0] = cvtpk(y[0], y[1]); w[1] = cvtpk(y[2], y[3]);
        *(u32x2*)(mid + (ng >> 1) * 1024 + (ng & 1) * 8) = w;
      }
    }
  }
}

extern "C" void kernel_launch(void* const* d_in, const int* in_sizes, int n_in, void* d_out, int out_size, void* d_ws, size_t ws_size,
                              hipStream_t stream) {
  const __bf16* x = (const __bf16*)d_in[0];
  const __bf16* ckv = (const __bf16*)d_in[1];
  const __bf16* wq = (const __bf16*)d_in[2];
  const __bf16* wuk = (const __bf16*)d_in[3];
  const __bf16* wuv = (const __bf16*)d_in[4];
  const __bf16* wo = (const __bf16*)d_in[5];
  const __bf16* w1 = (const __bf16*)d_in[7];
  const __bf16* b1 = (const __bf16*)d_in[8];
  const __bf16* w2 = (const __bf16*)d_in[9];
  const __bf16* b2 = (const __bf16*)d_in[10];
  char* ws = (char*)d_ws;
  (void)hipFuncSetAttribute((const void*)k1_kvproj, hipFuncAttributeMaxDynamicSharedMemorySize, K1_LDS);
  (void)hipFuncSetAttribute((const void*)k2_attn, hipFuncAttributeMaxDynamicSharedMemorySize, K2_LDS);
  (void)hipFuncSetAttribute((const void*)k35_gemm<0>, hipFuncAttributeMaxDynamicSharedMemorySize, K3_LDS);
  (void)hipFuncSetAttribute((const void*)k35_gemm<1>, hipFuncAttributeMaxDynamicSharedMemorySize, K3_LDS);
  (void)hipFuncSetAttribute((const void*)k4_ffn1, hipFuncAttributeMaxDynamicSharedMemorySize, K4_LDS);
  k1_kvproj<<<256, 512, K1_LDS, stream>>>(x, ckv, wuk, wuv, w1, ws);
  k2_attn<<<256, 512, K2_LDS, stream>>>(wq, wo, ws);
  k35_gemm<0><<<256, 256, K3_LDS, stream>>>(x, b2, w1, w2, (__bf16*)d_out, ws);
  k4_ffn1<<<256, 256, K4_LDS, stream>>>(b1, ws);
  k35_gemm<1><<<256, 256, K3_LDS, stream>>>(x, b2, w1, w2, (__bf16*)d_out, ws);
}
```

```cpp
#include <hip/hip_runtime.h>
#include <hip/hip_bf16.h>
#include <stdint.h>
#include <type_traits>

typedef __bf16 bf16x8 __attribute__((ext_vector_type(8)));
typedef __bf16 bf16x4 __attribute__((ext_vector_type(4)));
typedef __bf16 bf16x2 __attribute__((ext_vector_type(2)));
typedef short s16x4 __attribute__((ext_vector_type(4)));
typedef short s16x8 __attribute__((ext_vector_type(8)));
typedef float f32x16 __attribute__((ext_vector_type(16)));
typedef float f32x4 __attribute__((ext_vector_type(4)));
typedef float f32x2 __attribute__((ext_vector_type(2)));
typedef unsigned u32x4 __attribute__((ext_vector_type(4)));
typedef unsigned u32x2 __attribute__((ext_vector_type(2)));
typedef __amdgpu_buffer_rsrc_t rsrc_t;
#define LAS __attribute__((address_space(3)))
#define DI __device__ __forceinline__
#define SBAR() __builtin_amdgcn_sched_barrier(0)

constexpr int SEQ = 2048, NH = 32, HD = 128, DM = 640, DC = 512, FF = 1728, HDT = NH * HD;

#define CTS 128
constexpr int CST = CTS;
constexpr size_t WS_XNF = 0;
constexpr size_t WS_KVF = WS_XNF + (size_t)SEQ * DM * 2;
constexpr size_t WS_CTX = WS_KVF + (size_t)SEQ * HDT * 4;
constexpr size_t WS_WOF = WS_CTX + (size_t)136 * 128 * 1024;
constexpr size_t WS_W1F = WS_WOF + (size_t)136 * 40 * 1024;
constexpr size_t WS_W2F = WS_W1F + (size_t)DM * FF * 2;
constexpr size_t WS_HF  = WS_W2F + (size_t)DM * FF * 2;
constexpr size_t WS_MID = WS_HF + (size_t)SEQ * DM * 2;
constexpr size_t WS_ST  = WS_MID + (size_t)SEQ * FF * 2;
constexpr size_t WS_CS1 = WS_ST + (size_t)SEQ * 8 * 8;
constexpr size_t WS_CSP = WS_CS1 + 8192;
constexpr size_t WS_CNT = WS_CSP + (size_t)20 * FF * 4;
constexpr size_t WS_END = WS_CNT + 16384;
#define T_R0 24
#define T_PD 3
#define T_W2EARLY 8

template <int I, int N, class F> DI void static_for(F&& f) {
  if constexpr (I < N) { f(std::integral_constant<int, I>{}); static_for<I + 1, N>(f); }
}
template <int N> DI void wait_vm() { asm volatile("s_waitcnt vmcnt(%0)" ::"i"(N) : "memory"); }
DI void bar() { asm volatile("s_waitcnt lgkmcnt(0)\n\ts_barrier" ::: "memory"); }
DI rsrc_t mk_rsrc(const void* p, size_t bytes) { return __builtin_amdgcn_make_buffer_rsrc((void*)p, (short)0, (int)bytes, 0x00020000); }
DI void dma16(rsrc_t srd, unsigned voff, unsigned soff, unsigned lds_addr) {
  asm volatile("s_mov_b32 m0, %0\n\ts_nop 0\n\tbuffer_load_dwordx4 %1, %2, %3 offen lds" ::"s"(lds_addr), "v"(voff), "s"(srd), "s"(soff) : "memory");
}
DI void dma16x4(rsrc_t srd, unsigned voff, unsigned soff, unsigned lds_addr) {
  asm volatile("s_mov_b32 m0, %0\n\ts_nop 0\n\t"
               "buffer_load_dwordx4 %1, %2, %3 offen lds\n\t"
               "buffer_load_dwordx4 %1, %2, %3 offen offset:1024 lds\n\t"
               "buffer_load_dwordx4 %1, %2, %3 offen offset:2048 lds\n\t"
               "buffer_load_dwordx4 %1, %2, %3 offen offset:3072 lds" ::"s"(lds_addr), "v"(voff), "s"(srd), "s"(soff) : "memory");
}
DI void warm4(rsrc_t srd, unsigned voff, unsigned soff, unsigned lds_addr) {
  asm volatile("s_mov_b32 m0, %0\n\ts_nop 0\n\tbuffer_load_dword %1, %2, %3 offen lds" ::"s"(lds_addr), "v"(voff), "s"(srd), "s"(soff) : "memory");
}
template <int IMM> DI void gld16(bf16x8& dst, rsrc_t srd, unsigned voff, unsigned soff) {
  asm volatile("buffer_load_dwordx4 %0, %1, %2, %3 offen offset:%4" : "=v"(dst) : "v"(voff), "s"(srd), "s"(soff), "i"(IMM) : "memory");
}
template <int N> DI void wait_bar() { asm volatile("s_waitcnt vmcnt(%0) lgkmcnt(0)\n\ts_barrier" ::"i"(N) : "memory"); }
template <class T> DI void opaque(T& v) { asm volatile("" : "+v"(v)); }
DI unsigned sop(unsigned v) { asm volatile("" : "+s"(v)); return v; }
DI unsigned cvtpk(float lo, float hi) { f32x2 v = {lo, hi}; bf16x2 b = __builtin_convertvector(v, bf16x2); return __builtin_bit_cast(unsigned, b); }
DI bf16x8 lds_rd16(LAS const char* p) { return *(LAS const bf16x8*)p; }
DI s16x4 lds_tr(LAS const char* p) { return __builtin_amdgcn_ds_read_tr16_b64_v4i16((LAS s16x4*)p); }
DI bf16x8 lds_tr2(LAS const char* p0, LAS const char* p1) {
  s16x4 a = lds_tr(p0), b = lds_tr(p1);
  s16x8 c = __builtin_shufflevector(a, b, 0, 1, 2, 3, 4, 5, 6, 7);
  return __builtin_bit_cast(bf16x8, c);
}
DI float bf_lo(unsigned u) { return __uint_as_float(u << 16); }
DI float bf_hi(unsigned u) { return __uint_as_float(u & 0xffff0000u); }
#define MFMA16(a, b, c) __builtin_amdgcn_mfma_f32_16x16x32_bf16(a, b, c, 0, 0, 0)
DI u32x4 pack8(const f32x16& x, int s, float sc) {
  u32x4 w;
#pragma unroll
  for (int i = 0; i < 4; ++i) w[i] = cvtpk(x[8 * s + 2 * i] * sc, x[8 * s + 2 * i + 1] * sc);
  return w;
}
template <int CTRL> DI float dpp_add(float v) {
  return v + __int_as_float(__builtin_amdgcn_update_dpp(0, __float_as_int(v), CTRL, 0xf, 0xf, true));
}
DI float quad16_sum(float v) {
  auto r = __builtin_amdgcn_permlane16_swap(__float_as_uint(v), __float_as_uint(v), false, false);
  v = __uint_as_float(r[0]) + __uint_as_float(r[1]);
  auto q = __builtin_amdgcn_permlane32_swap(__float_as_uint(v), __float_as_uint(v), false, false);
  return __uint_as_float(q[0]) + __uint_as_float(q[1]);
}
DI float swap32_sum(float v) {
  auto r = __builtin_amdgcn_permlane32_swap(__float_as_uint(v), __float_as_uint(v), false, false);
  return __uint_as_float(r[0]) + __uint_as_float(r[1]);
}

constexpr int ST_AUX = 16, KV_ST_AUX = 16;
#define ST_FLAG " sc1"
DI void st16(void* p, u32x4 v) { asm volatile("global_store_dwordx4 %0, %1, off" ST_FLAG "\n\ts_nop 1" ::"v"(p), "v"(v) : "memory"); }
DI u32x4 pair_rows(u32x2 a, u32x2 b) {
  auto r0 = __builtin_amdgcn_permlane16_swap(a[0], b[0], false, false);
  auto r1 = __builtin_amdgcn_permlane16_swap(a[1], b[1], false, false);
  return (u32x4){r0[0], r1[0], r0[1], r1[1]};
}
DI void rows16_to_32(u32x4& x0, u32x4& x1) {
#pragma unroll
  for (int e = 0; e < 4; ++e) {
    auto a = __builtin_amdgcn_permlane16_swap(x0[e], x1[e], false, false);
    auto b = __builtin_amdgcn_permlane32_swap(a[0], a[1], false, false);
    x0[e] = b[0]; x1[e] = b[1];
  }
}
DI u32x4 pack44(const f32x4& a, const f32x4& b, float sc) { return (u32x4){cvtpk(a[0] * sc, a[1] * sc), cvtpk(a[2] * sc, a[3] * sc), cvtpk(b[0] * sc, b[1] * sc), cvtpk(b[2] * sc, b[3] * sc)}; }
DI u32x4 pack44n(const f32x4& a, const f32x4& b) { return (u32x4){cvtpk(a[0], a[1]), cvtpk(a[2], a[3]), cvtpk(b[0], b[1]), cvtpk(b[2], b[3])}; }

DI void relayout_issue(rsrc_t srd, unsigned ldN  , unsigned k0, unsigned n0, unsigned lds_addr, int lane) {
  const unsigned voff = (((lane >> 5) * 4 + ((lane >> 2) & 3)) * ldN + ((lane >> 4) & 1) * 32 + (lane & 3) * 8) * 2;
#pragma unroll
  for (int p = 0; p < 4; ++p) dma16(srd, voff, ((k0 + 8 * p) * ldN + n0) * 2, lds_addr + p * 1024);
}
DI void relayout_finish(int perm, LAS const char* img, char* dst  , size_t ng_stride, int lane, float* csum) {
  const int g = lane >> 4, q = (lane >> 2) & 3, p = lane & 3;
  const int rg0 = perm == 0 ? 4 * (g >> 1) + (g & 1) : 2 * g;
  const int rg1 = perm == 0 ? rg0 + 2 : 2 * g + 1;
  bf16x8 f[4];
#pragma unroll
  for (int i = 0; i < 4; ++i) {
    const int o = (i >> 1) * 256 + q * 64 + (i & 1) * 32 + p * 8;
    f[i] = lds_tr2(img + rg0 * 512 + o, img + rg1 * 512 + o);
  }
#pragma unroll
  for (int i = 0; i < 4; ++i) st16(dst + i * ng_stride + lane * 16, __builtin_bit_cast(u32x4, f[i]));
  if (csum) {
    float c[4];
#pragma unroll
    for (int i = 0; i < 4; ++i) {
      const u32x4 u = __builtin_bit_cast(u32x4, f[i]);
      float t = ((bf_lo(u[0]) + bf_hi(u[0])) + (bf_lo(u[1]) + bf_hi(u[1]))) + ((bf_lo(u[2]) + bf_hi(u[2])) + (bf_lo(u[3]) + bf_hi(u[3])));
      c[i] = quad16_sum(t);
    }
    const float v = g == 0 ? c[0] : (g == 1 ? c[1] : (g == 2 ? c[2] : c[3]));
    csum[lane] = v;
  }
}

constexpr int K1_SLOT = 49152, K1_WARM = 3 * K1_SLOT, K1_PD = 4;
constexpr int k1_stage(int i) { return i < 128 ? i / 16 : 8 + (i - 128) / 32; }
constexpr bool k1_first(int i) { return i < 128 ? (i % 16 == 0) : ((i - 128) % 32 == 0); }
constexpr int k1_coff(int i) {
  if (i < 128) return ((i >> 1) & 3) * 4096;
  const int k = (i - 128) % 32, s = k1_stage(i);
  if (s == 11) return (k >> 2) < 4 ? K1_SLOT + (k >> 2) * 4096 : ((k >> 2) - 4) * 4096;
  return (k >> 2) * 4096;
}
constexpr int k1_base(int s) { return s == 11 ? 32768 : (s % 3) * K1_SLOT; }
#define MERGE_RL_EARLY 0
#define PRIO_KV 1
#define K1_WQ 93
#define K1_SPREAD 2
constexpr int k1_wait(int s) { return (s == K1_WQ / 16 + 1 || s == K1_WQ / 16 + 2 ? 1 : 0) + (s <= 6 ? 6 : s <= 8 ? 4 : 8 + (32 * (s - 8) + 128 - K1_PD - 131 + 7) / 8); }

template <bool IS_V, class FW, class FX>
DI void kv_body(LAS char* lds, rsrc_t sC, rsrc_t sWk, rsrc_t sWv, rsrc_t sKV, rsrc_t sWq, int h, int kb, int wid, int lane, unsigned vW, FW&& nextW, FX&& nextX) {
  const unsigned ldsb = (unsigned)(size_t)lds;
  const int g = lane >> 4, db = wid & 3;
  const int flip = ((h >> 3) & 1) * 128;
  const unsigned key0 = kb * 256;
  const unsigned vC = (unsigned)(((lane >> 3) * DC + (((lane & 7) ^ (4 * ((wid ^ (wid >> 1)) & 1) + (lane >> 4))) * 8)) * 2);
  auto piece = [&](auto S_, auto P_) {
    constexpr int s = decltype(S_)::value, pi = decltype(P_)::value;
    constexpr unsigned slot = (s % 3) * K1_SLOT;
    if constexpr (s < 8) {
      const unsigned p = wid + 8 * (pi / 3), cbk = s * 64;
      if constexpr (pi % 3 == 0) dma16(sC, vC, ((key0 + flip + 8 * p) * DC + cbk) * 2, ldsb + slot + p * 1024);
      else dma16(pi % 3 == 1 ? sWk : sWv, vW, ((cbk + 4 * p) * HDT + h * HD) * 2, ldsb + slot + (pi % 3) * 16384 + p * 1024);
    } else if constexpr (pi < 4) {
      const unsigned pr = wid & 3, cs = (wid >> 2) + 2 * pi;
      const unsigned dst = s == 11 ? (pi < 2 ? K1_SLOT + 32768 + cs * 4096 : 32768 + (cs - 4) * 4096) : slot + cs * 4096;
      dma16(sC, vC, ((key0 + (128 - flip) + 32 * (s - 8) + 8 * pr) * DC + cs * 64) * 2, ldsb + dst + pr * 1024);
    }
  };
  auto issue = [&](auto S_) { static_for<0, 6>([&](auto P_) { piece(S_, P_); }); };
  issue(std::integral_constant<int, 0>{});
  __builtin_amdgcn_s_barrier();
  issue(std::integral_constant<int, 1>{});
  const int rho = lane & 15, row0 = IS_V ? 16 * (rho >> 3) + (rho & 7) : rho;
  unsigned cb00 = ldsb + row0 * 128 + ((g ^ ((row0 >> 1) & 7) ^ ((row0 >> 2) & 4)) * 16);
  unsigned wb0 = ldsb + 16384 + (IS_V ? 16384 : 0) + g * 2048 + ((lane >> 2) & 3) * 64 + (g & 1) * 32 + (lane & 3) * 8 + db * 256;
  const unsigned vout = lane * 16 + db * 2048 + (IS_V ? 16384 : 0);
  bf16x8 wf[32], cf[K1_PD + 1];
  f32x4 acc[8][2][2];
  u32x4 y0, y1;
  LAS const char* cb[2]; LAS const char* wb[2];
  auto store_half = [&](f32x4 (&a)[2][2], int kt  , int s) {
    if (s == 0) {
      if constexpr (IS_V) { y0 = pack44n(a[0][0], a[1][0]); y1 = pack44n(a[0][1], a[1][1]); }
      else { y0 = pack44n(a[0][0], a[0][1]); y1 = pack44n(a[1][0], a[1][1]); }
      rows16_to_32(y0, y1);
      opaque(y1);
    }
    const unsigned gk = kb * 8 + (kt ^ (flip >> 5));
    const unsigned so = (h * 32 + (gk >> 1)) * 32768 + (gk & 1) * 8192;
    __builtin_amdgcn_raw_buffer_store_b128(s ? y1 : y0, sKV, vout + s * 1024, so, KV_ST_AUX);
  };
  static_for<-K1_PD, 256>([&](auto I_) {
    constexpr int i = decltype(I_)::value, r = i + K1_PD;
    if constexpr (r < 256) {
      constexpr int sr = k1_stage(r);
      if constexpr (k1_first(r)) {
        wait_bar<k1_wait(sr)>();
        if constexpr (sr == 8 || (!K1_SPREAD && sr < 8)) issue(std::integral_constant<int, sr + 2>{});
        if constexpr (sr == 8) issue(std::integral_constant<int, 11>{});
        if constexpr (sr == 9 || sr == 10) nextW(sr - 9);
#pragma unroll
        for (int k = 0; k < 2; ++k) { opaque(cb00); cb[k] = (LAS const char*)(size_t)((cb00 + k1_base(sr)) ^ (k << 6)); opaque(cb[k]); }
        if constexpr (sr < 8) {
#pragma unroll
          for (int k = 0; k < 2; ++k) { opaque(wb0); wb[k] = (LAS const char*)(size_t)((wb0 + k1_base(sr)) ^ (k << 5)); opaque(wb[k]); }
        }
      }
      if constexpr (PRIO_KV) { constexpr int q4 = r < 128 ? 4 : 8; if constexpr (r % q4 == 0) __builtin_amdgcn_s_setprio(3 - (r / q4) % 4); }
      if constexpr (K1_SPREAD && r < 128 && r % 16 < 6 * K1_SPREAD && (r % 16) % K1_SPREAD == 0) piece(std::integral_constant<int, sr + 2>{}, std::integral_constant<int, (r % 16) / K1_SPREAD>{});
      if constexpr (r == K1_WQ) {
          const unsigned ln = (unsigned)(kb * 8 + wid) * 20 + (lane < 20 ? lane : 0);
          warm4(sWq, (ln >> 1) * (HDT * 2) + (ln & 1) * 128, h * 256, ldsb + K1_WARM + wid * 256);
        }
      if constexpr (r < 128 && r % 8 == 0) {
#pragma unroll
        for (int dg = 0; dg < 2; ++dg) wf[r / 4 + dg] = lds_tr2(wb[dg] + ((r / 8) & 1) * 8192, wb[dg] + ((r / 8) & 1) * 8192 + 1024);
      }
      constexpr int kk = r < 128 ? (r >> 3) & 1 : (r >> 1) & 1, tt = r & 1;
      cf[r % (K1_PD + 1)] = lds_rd16(cb[kk ^ tt] + k1_coff(r) + tt * (IS_V ? 1024 : 2048));
    }
    if constexpr (i >= 0) {
      const bf16x8 c = cf[i % (K1_PD + 1)];
      constexpr int kg = i < 128 ? i / 8 : ((i - 128) % 32) / 2, b = i < 128 ? (i >> 1) & 3 : 4 + (i - 128) / 32, t = i & 1;
#pragma unroll
      for (int dg = 0; dg < 2; ++dg) {
        const f32x4 cin = kg == 0 ? f32x4{} : acc[b][t][dg];
        if constexpr (IS_V) acc[b][t][dg] = MFMA16(c, wf[2 * kg + dg], cin); else acc[b][t][dg] = MFMA16(wf[2 * kg + dg], c, cin);
      }
      if constexpr (i >= 128) {
        if constexpr ((i - 128) % 8 == 3 && (i - 128) / 8 < 14) store_half(acc[(i - 128) / 16], (i - 128) / 16, ((i - 128) / 8) & 1);
        if constexpr (i == 236) nextX(0);
        if constexpr (i == 255) { store_half(acc[7], 7, 0); store_half(acc[7], 7, 1); nextX(1); }
      }
    }
    SBAR();
  });
}

#define K0_NW 8
__global__ __launch_bounds__(384) void k0_prep(const __bf16* __restrict__ x, const __bf16* __restrict__ ckv, const __bf16* __restrict__ wuk,
                                               const __bf16* __restrict__ wuv, const __bf16* __restrict__ b1, const __bf16* __restrict__ b2, char* __restrict__ ws) {
  const int bid = blockIdx.x, tid = threadIdx.x, lane = tid & 63;
  const int wid = __builtin_amdgcn_readfirstlane(tid >> 6);
  if (wid < 2) {
    const int row = wid * 4 + (lane & 3), part = lane >> 2;
    const __bf16* xr = x + (size_t)(bid * 8 + row) * DM + part * 8;
    char* xnf = ws + WS_XNF + (size_t)(bid >> 1) * 20 * 1024 + (bid & 1) * 128;
    u32x4 u[5];
#pragma unroll
    for (int i = 0; i < 5; ++i) u[i] = *(const u32x4*)(xr + i * 128);
    float v[40];
#pragma unroll
    for (int i = 0; i < 5; ++i)
#pragma unroll
      for (int e = 0; e < 4; ++e) { v[8 * i + 2 * e] = bf_lo(u[i][e]); v[8 * i + 2 * e + 1] = bf_hi(u[i][e]); }
    auto rsum = [](float t) { t = dpp_add<0x124>(t); t = dpp_add<0x128>(t); return quad16_sum(t); };
    float s0 = 0, s1 = 0;
#pragma unroll
    for (int e = 0; e < 20; ++e) { s0 += v[e]; s1 += v[20 + e]; }
    const float mean = rsum(s0 + s1) * (1.0f / DM);
    float q0 = 0, q1 = 0;
#pragma unroll
    for (int e = 0; e < 20; ++e) { v[e] -= mean; v[20 + e] -= mean; q0 += v[e] * v[e]; q1 += v[20 + e] * v[20 + e]; }
    const float rstd = __builtin_amdgcn_rsqf(rsum(q0 + q1) * (1.0f / DM) + 1e-5f);
#pragma unroll
    for (int i = 0; i < 5; ++i) {
      u32x4 w;
#pragma unroll
      for (int e = 0; e < 4; ++e) w[e] = cvtpk(v[8 * i + 2 * e] * rstd, v[8 * i + 2 * e + 1] * rstd);
      const int c = part + 16 * i;
      st16(xnf + c * 256 + row * 16, w);
    }
  } else {
    const int idx = bid >> 3, h = (bid & 7) + 8 * (idx & 3), kb = idx >> 2, w = wid - 2;
    if (wid == 2 && bid < 8) { ((u32x4*)(ws + WS_CNT))[bid * 128 + lane] = u32x4{}; ((u32x4*)(ws + WS_CNT))[bid * 128 + 64 + lane] = u32x4{}; }
    const char* pw = (const char*)((w & 1) ? wuk : wuv) + (size_t)((lane >> 4) * 8 + kb + (w >> 1) * 32 * K0_NW) * (HDT * 2) + h * 256 + (lane & 15) * 16;
    const char* pc = (const char*)ckv + (size_t)(bid * 8 + w * 2) * (DC * 2) + lane * 16;
    u32x4 d[10], tc = {}; unsigned tb = 0;
#pragma unroll
    for (int i = 0; i < K0_NW; ++i) d[i] = *(const u32x4*)(pw + (size_t)i * 32 * (HDT * 2));
    d[8] = *(const u32x4*)pc; d[9] = *(const u32x4*)(pc + 1024);
    {
      unsigned long long c0, c1;
      asm volatile("s_getpc_b64 s[20:21]\n\ts_add_u32 s20, s20, k2_attn@rel32@lo+4\n\ts_addc_u32 s21, s21, k2_attn@rel32@hi+12" : "={s[20:21]}"(c0)::"scc");
      asm volatile("s_getpc_b64 s[22:23]\n\ts_add_u32 s22, s22, k9_end@rel32@lo+4\n\ts_addc_u32 s23, s23, k9_end@rel32@hi+12" : "={s[22:23]}"(c1)::"scc");
      const unsigned long long o = (unsigned long long)(idx * 4 + w) * 1024 + lane * 16;
      if (c1 > c0 && o + 16 <= c1 - c0) tc = *(const __attribute__((address_space(1))) u32x4*)(c0 + o);
    }
    if (bid == 9 && wid == 3 && lane < 37) tb = *(const unsigned*)(lane < 27 ? b1 + lane * 64 : b2 + (lane - 27) * 64);
    asm volatile("" ::"v"(d[8]), "v"(d[9]), "v"(tc), "v"(tb));
#pragma unroll
    for (int i = 0; i < K0_NW; ++i) asm volatile("" ::"v"(d[i]));
  }
}

#define ATTN_ASM \
  "s_cmp_eq_u32 s40, 0\n" \
  "s_cbranch_scc1 FIN_%=\n" \
  "ds_read_b128 v[160:163], v200 offset:0\n" \
  "ds_read_b128 v[164:167], v200 offset:1024\n" \
  "ds_read_b128 v[168:171], v200 offset:2048\n" \
  "ds_read_b128 v[172:175], v200 offset:3072\n" \
  "ds_read_b128 v[176:179], v200 offset:4096\n" \
  "ds_read_b128 v[180:183], v200 offset:5120\n" \
  "ds_read_b128 v[184:187], v200 offset:6144\n" \
  "ds_read_b128 v[188:191], v200 offset:7168\n" \
  "v_mov_b32 v220, 0xff800000\n" \
  "v_mov_b32 v213, 0\n" \
  "s_mov_b64 s[52:53], 0\n" \
  "s_mov_b32 s55, 0x44800000\n" \
  "s_add_u32 s71, s49, 32\n" \
  "s_waitcnt lgkmcnt(7)\n" \
  "v_mfma_f32_32x32x16_bf16 v[64:79], v[160:163], v[112:115], v[96:111]\n" \
  "s_waitcnt lgkmcnt(6)\n" \
  "v_mfma_f32_32x32x16_bf16 v[64:79], v[164:167], v[116:119], v[64:79]\n" \
  "s_waitcnt lgkmcnt(5)\n" \
  "v_mfma_f32_32x32x16_bf16 v[64:79], v[168:171], v[120:123], v[64:79]\n" \
  "s_waitcnt lgkmcnt(4)\n" \
  "v_mfma_f32_32x32x16_bf16 v[64:79], v[172:175], v[124:127], v[64:79]\n" \
  "s_waitcnt lgkmcnt(3)\n" \
  "v_mfma_f32_32x32x16_bf16 v[64:79], v[176:179], v[128:131], v[64:79]\n" \
  "s_waitcnt lgkmcnt(2)\n" \
  "v_mfma_f32_32x32x16_bf16 v[64:79], v[180:183], v[132:135], v[64:79]\n" \
  "s_waitcnt lgkmcnt(1)\n" \
  "v_mfma_f32_32x32x16_bf16 v[64:79], v[184:187], v[136:139], v[64:79]\n" \
  "s_waitcnt lgkmcnt(0)\n" \
  "v_mfma_f32_32x32x16_bf16 v[64:79], v[188:191], v[140:143], v[64:79]\n" \
  "ds_read_b128 v[192:195], v200 offset:8192\n" \
  "ds_read_b128 v[164:167], v200 offset:9216\n" \
  "ds_read_b128 v[196:199], v200 offset:10240\n" \
  "ds_read_b128 v[168:171], v200 offset:11264\n" \
  "ds_read_b128 v[172:175], v200 offset:12288\n" \
  "ds_read_b128 v[176:179], v200 offset:13312\n" \
  "ds_read_b128 v[180:183], v200 offset:14336\n" \
  "ds_read_b128 v[184:187], v200 offset:15360\n" \
  "s_nop 7\n" \
  "s_cmp_gt_i32 s47, s71\n" \
  "s_cbranch_scc1 MASKE_%=\n" \
  "MRETE_%=:\n" \
  "s_cmp_eq_u64 s[50:51], 0\n" \
  "s_cbranch_scc1 NOFIRST_%=\n" \
  "v_max3_f32 v216, v64, v65, v66\n" \
  "v_max3_f32 v217, v67, v68, v69\n" \
  "v_max3_f32 v216, v216, v70, v71\n" \
  "v_max3_f32 v217, v217, v72, v73\n" \
  "v_max3_f32 v216, v216, v74, v75\n" \
  "v_max3_f32 v217, v217, v76, v77\n" \
  "v_max3_f32 v216, v216, v78, v79\n" \
  "v_max_f32 v216, v216, v217\n" \
  "v_mov_b32 v217, v216\n" \
  "s_nop 1\n" \
  "v_permlane32_swap_b32 v216, v217\n" \
  "s_nop 1\n" \
  "v_max_f32 v216, v216, v217\n" \
  "v_cmp_lt_f32 vcc, s54, v216\n" \
  "s_nop 1\n" \
  "v_cndmask_b32 v216, 0, v216, vcc\n" \
  "v_sub_f32 v96, 0, v216\n" \
  "v_sub_f32 v64, v64, v216\n" \
  "v_sub_f32 v65, v65, v216\n" \
  "v_sub_f32 v66, v66, v216\n" \
  "v_sub_f32 v67, v67, v216\n" \
  "v_sub_f32 v68, v68, v216\n" \
  "v_sub_f32 v69, v69, v216\n" \
  "v_sub_f32 v70, v70, v216\n" \
  "v_sub_f32 v71, v71, v216\n" \
  "v_sub_f32 v72, v72, v216\n" \
  "v_sub_f32 v73, v73, v216\n" \
  "v_sub_f32 v74, v74, v216\n" \
  "v_sub_f32 v75, v75, v216\n" \
  "v_sub_f32 v76, v76, v216\n" \
  "v_sub_f32 v77, v77, v216\n" \
  "v_sub_f32 v78, v78, v216\n" \
  "v_sub_f32 v79, v79, v216\n" \
  "v_mov_b32 v97, v96\n" \
  "v_mov_b32 v98, v96\n" \
  "v_mov_b32 v99, v96\n" \
  "v_mov_b32 v100, v96\n" \
  "v_mov_b32 v101, v96\n" \
  "v_mov_b32 v102, v96\n" \
  "v_mov_b32 v103, v96\n" \
  "v_mov_b32 v104, v96\n" \
  "v_mov_b32 v105, v96\n" \
  "v_mov_b32 v106, v96\n" \
  "v_mov_b32 v107, v96\n" \
  "v_mov_b32 v108, v96\n" \
  "v_mov_b32 v109, v96\n" \
  "v_mov_b32 v110, v96\n" \
  "v_mov_b32 v111, v96\n" \
  "s_mov_b64 s[50:51], 0\n" \
  "s_mov_b32 s54, 4.0\n" \
  "NOFIRST_%=:\n" \
  "s_setprio 0\n" \
  "v_exp_f32 v207, v64\n" \
  "v_exp_f32 v208, v65\n" \
  "v_exp_f32 v209, v66\n" \
  "v_cvt_pk_bf16_f32 v144, v207, v208\n" \
  "v_exp_f32 v210, v67\n" \
  "v_add_f32 v205, v207, v208\n" \
  "v_exp_f32 v211, v68\n" \
  "v_cvt_pk_bf16_f32 v145, v209, v210\n" \
  "v_exp_f32 v212, v69\n" \
  "v_add_f32 v206, v209, v210\n" \
  "v_exp_f32 v207, v70\n" \
  "v_cvt_pk_bf16_f32 v146, v211, v212\n" \
  "v_exp_f32 v208, v71\n" \
  "v_add_f32 v205, v205, v211\n" \
  "v_add_f32 v206, v206, v212\n" \
  "v_exp_f32 v209, v72\n" \
  "v_cvt_pk_bf16_f32 v147, v207, v208\n" \
  "s_mov_b32 s70, s69\n" \
  "s_cmp_lt_i32 s41, 0\n" \
  "s_cselect_b32 s69, 1, 2\n" \
  "v_exp_f32 v210, v73\n" \
  "v_add_f32 v205, v205, v207\n" \
  "v_add_f32 v206, v206, v208\n" \
  "s_cmp_lt_i32 s42, 1\n" \
  "s_cselect_b32 s69, 0, s69\n" \
  "v_exp_f32 v211, v74\n" \
  "v_cvt_pk_bf16_f32 v148, v209, v210\n" \
  "v_exp_f32 v212, v75\n" \
  "s_waitcnt lgkmcnt(0)\n" \
  "s_branch MID_%=\n" \
  "LOOP_%=:\n" \
  "s_waitcnt lgkmcnt(5)\n" \
  "v_mfma_f32_32x32x16_bf16 v[64:79], v[160:163], v[112:115], v[96:111]\n" \
  "ds_read_b128 v[160:163], v200 offset:7168\n" \
  "v_add_f32 v206, v206, v212\n" \
  "v_exp_f32 v209, v88\n" \
  "v_cvt_pk_bf16_f32 v155, v207, v208\n" \
  "v_exp_f32 v210, v89\n" \
  "v_mfma_f32_32x32x16_bf16 v[64:79], v[164:167], v[116:119], v[64:79]\n" \
  "ds_read_b128 v[164:167], v202 offset:8192\n" \
  "s_setprio 1\n" \
  "v_add_f32 v205, v205, v207\n" \
  "v_add_f32 v206, v206, v208\n" \
  "v_exp_f32 v211, v90\n" \
  "s_waitcnt lgkmcnt(5)\n" \
  "v_mfma_f32_32x32x16_bf16 v[64:79], v[168:171], v[120:123], v[64:79]\n" \
  "ds_read_b128 v[168:171], v202 offset:10240\n" \
  "v_cvt_pk_bf16_f32 v156, v209, v210\n" \
  "v_exp_f32 v212, v91\n" \
  "v_add_f32 v205, v205, v209\n" \
  "v_mfma_f32_32x32x16_bf16 v[64:79], v[172:175], v[124:127], v[64:79]\n" \
  "ds_read_b128 v[172:175], v202 offset:12288\n" \
  "v_add_f32 v206, v206, v210\n" \
  "v_exp_f32 v207, v92\n" \
  "v_cvt_pk_bf16_f32 v157, v211, v212\n" \
  "v_exp_f32 v208, v93\n" \
  "s_waitcnt lgkmcnt(5)\n" \
  "v_mfma_f32_32x32x16_bf16 v[64:79], v[176:179], v[128:131], v[64:79]\n" \
  "ds_read_b128 v[176:179], v202 offset:14336\n" \
  "v_add_f32 v205, v205, v211\n" \
  "v_add_f32 v206, v206, v212\n" \
  "v_exp_f32 v209, v94\n" \
  "v_mfma_f32_32x32x16_bf16 v[64:79], v[180:183], v[132:135], v[64:79]\n" \
  "ds_read_b128 v[180:183], v202 offset:9216\n" \
  "v_cvt_pk_bf16_f32 v158, v207, v208\n" \
  "v_exp_f32 v210, v95\n" \
  "v_add_f32 v205, v205, v207\n" \
  "v_add_f32 v206, v206, v208\n" \
  "s_waitcnt lgkmcnt(5)\n" \
  "v_mfma_f32_32x32x16_bf16 v[64:79], v[184:187], v[136:139], v[64:79]\n" \
  "ds_read_b128 v[184:187], v202 offset:11264\n" \
  "ds_read_b128 v[188:191], v202 offset:13312\n" \
  "s_nop 0\n" \
  "v_cvt_pk_bf16_f32 v159, v209, v210\n" \
  "v_add_f32 v205, v205, v209\n" \
  "v_add_f32 v206, v206, v210\n" \
  "v_mfma_f32_32x32x16_bf16 v[64:79], v[160:163], v[140:143], v[64:79]\n" \
  "ds_read_b128 v[160:163], v202 offset:15360\n" \
  "ds_read_b128 v[192:195], v200 offset:8192\n" \
  "v_add_f32 v205, v205, v206\n" \
  "v_cmp_nge_f32 vcc, s55, v205\n" \
  "s_or_b64 s[62:63], vcc, s[52:53]\n" \
  "s_cbranch_scc1 RAREB_%=\n" \
  "RETB_%=:\n" \
  "s_waitcnt lgkmcnt(7)\n" \
  "v_mfma_f32_32x32x16_bf16 v[0:15], v[164:167], v[152:155], v[0:15]\n" \
  "ds_read_b128 v[164:167], v200 offset:9216\n" \
  "ds_read_b128 v[196:199], v200 offset:10240\n" \
  "v_add_f32 v204, v204, v205\n" \
  "s_cmp_gt_i32 s47, s71\n" \
  "s_cbranch_scc1 MASKA_%=\n" \
  "MRETA_%=:\n" \
  "v_mfma_f32_32x32x16_bf16 v[16:31], v[168:171], v[152:155], v[16:31]\n" \
  "ds_read_b128 v[168:171], v200 offset:11264\n" \
  "s_setprio 0\n" \
  "v_exp_f32 v207, v64\n" \
  "v_exp_f32 v208, v65\n" \
  "s_waitcnt lgkmcnt(8)\n" \
  "v_mfma_f32_32x32x16_bf16 v[32:47], v[172:175], v[152:155], v[32:47]\n" \
  "ds_read_b128 v[172:175], v200 offset:12288\n" \
  "v_exp_f32 v209, v66\n" \
  "v_cvt_pk_bf16_f32 v144, v207, v208\n" \
  "v_exp_f32 v210, v67\n" \
  "v_add_f32 v205, v207, v208\n" \
  "v_mfma_f32_32x32x16_bf16 v[48:63], v[176:179], v[152:155], v[48:63]\n" \
  "ds_read_b128 v[176:179], v200 offset:13312\n" \
  "v_exp_f32 v211, v68\n" \
  "v_cvt_pk_bf16_f32 v145, v209, v210\n" \
  "v_exp_f32 v212, v69\n" \
  "v_add_f32 v206, v209, v210\n" \
  "s_waitcnt lgkmcnt(8)\n" \
  "v_mfma_f32_32x32x16_bf16 v[0:15], v[180:183], v[156:159], v[0:15]\n" \
  "ds_read_b128 v[180:183], v200 offset:14336\n" \
  "v_exp_f32 v207, v70\n" \
  "v_cvt_pk_bf16_f32 v146, v211, v212\n" \
  "v_exp_f32 v208, v71\n" \
  "v_mfma_f32_32x32x16_bf16 v[16:31], v[184:187], v[156:159], v[16:31]\n" \
  "ds_read_b128 v[184:187], v200 offset:15360\n" \
  "v_add_f32 v205, v205, v211\n" \
  "v_add_f32 v206, v206, v212\n" \
  "v_exp_f32 v209, v72\n" \
  "v_cvt_pk_bf16_f32 v147, v207, v208\n" \
  "s_waitcnt lgkmcnt(8)\n" \
  "v_mfma_f32_32x32x16_bf16 v[32:47], v[188:191], v[156:159], v[32:47]\n" \
  "s_mov_b32 s70, s69\n" \
  "s_cmp_lt_i32 s41, 0\n" \
  "s_cselect_b32 s69, 1, 2\n" \
  "v_exp_f32 v210, v73\n" \
  "v_add_f32 v205, v205, v207\n" \
  "v_add_f32 v206, v206, v208\n" \
  "v_mfma_f32_32x32x16_bf16 v[48:63], v[160:163], v[156:159], v[48:63]\n" \
  "s_cmp_lt_i32 s42, 1\n" \
  "s_cselect_b32 s69, 0, s69\n" \
  "v_exp_f32 v211, v74\n" \
  "v_cvt_pk_bf16_f32 v148, v209, v210\n" \
  "v_exp_f32 v212, v75\n" \
  "MID_%=:\n" \
  "s_waitcnt lgkmcnt(6)\n" \
  "v_mfma_f32_32x32x16_bf16 v[80:95], v[192:195], v[112:115], v[96:111]\n" \
  "s_add_u32 s41, s41, 1\n" \
  "s_sub_u32 s42, s42, 1\n" \
  "v_add_f32 v205, v205, v209\n" \
  "v_add_f32 v206, v206, v210\n" \
  "v_exp_f32 v207, v76\n" \
  "v_mfma_f32_32x32x16_bf16 v[80:95], v[164:167], v[116:119], v[80:95]\n" \
  "s_waitcnt vmcnt(0) lgkmcnt(0)\n" \
  "s_barrier\n" \
  "s_setprio 3\n" \
  "ds_read_b128 v[160:163], v203 offset:0\n" \
  "ds_read_b128 v[164:167], v203 offset:2048\n" \
  "v_cvt_pk_bf16_f32 v149, v211, v212\n" \
  "v_mfma_f32_32x32x16_bf16 v[80:95], v[196:199], v[120:123], v[80:95]\n" \
  "ds_read_b128 v[188:191], v203 offset:4096\n" \
  "s_cmp_lt_u32 s69, 1\n" \
  "s_cbranch_scc1 DK0E_%=\n" \
  "s_mov_b32 m0, s45\n" \
  "s_nop 0\n" \
  "buffer_load_dwordx4 v215, s[64:67], s43 offen sc1 lds\n" \
  "buffer_load_dwordx4 v215, s[64:67], s43 offen offset:1024 sc1 lds\n" \
  "s_add_u32 s43, s43, 0x8000\n" \
  "DK0E_%=:\n" \
  "v_mfma_f32_32x32x16_bf16 v[80:95], v[168:171], v[124:127], v[80:95]\n" \
  "ds_read_b128 v[168:171], v203 offset:6144\n" \
  "s_cmp_eq_i32 s42, -1\n" \
  "s_cbranch_scc1 RL_%=\n" \
  "RLRET_%=:\n" \
  "v_exp_f32 v208, v77\n" \
  "v_add_f32 v205, v205, v211\n" \
  "v_add_f32 v206, v206, v212\n" \
  "v_mfma_f32_32x32x16_bf16 v[80:95], v[172:175], v[128:131], v[80:95]\n" \
  "s_cmp_lt_u32 s69, 2\n" \
  "s_cbranch_scc1 DK1E_%=\n" \
  "s_add_u32 m0, s45, 0x4000\n" \
  "s_nop 0\n" \
  "buffer_load_dwordx4 v215, s[64:67], s43 offen sc1 lds\n" \
  "buffer_load_dwordx4 v215, s[64:67], s43 offen offset:1024 sc1 lds\n" \
  "s_add_u32 s43, s43, 0x8000\n" \
  "DK1E_%=:\n" \
  "v_mfma_f32_32x32x16_bf16 v[80:95], v[176:179], v[132:135], v[80:95]\n" \
  "ds_read_b128 v[172:175], v203 offset:1024\n" \
  "s_cmp_eq_i32 s42, s87\n" \
  "s_cbranch_scc1 WRM_%=\n" \
  "WRMRET_%=:\n" \
  "v_exp_f32 v209, v78\n" \
  "v_cvt_pk_bf16_f32 v150, v207, v208\n" \
  "v_mfma_f32_32x32x16_bf16 v[80:95], v[180:183], v[136:139], v[80:95]\n" \
  "ds_read_b128 v[176:179], v203 offset:3072\n" \
  "v_exp_f32 v210, v79\n" \
  "v_add_f32 v205, v205, v207\n" \
  "v_add_f32 v206, v206, v208\n" \
  "s_nop 0\n" \
  "v_cvt_pk_bf16_f32 v151, v209, v210\n" \
  "v_mfma_f32_32x32x16_bf16 v[80:95], v[184:187], v[140:143], v[80:95]\n" \
  "ds_read_b128 v[180:183], v203 offset:5120\n" \
  "v_add_f32 v205, v205, v209\n" \
  "v_add_f32 v206, v206, v210\n" \
  "v_add_f32 v205, v205, v206\n" \
  "v_cmp_nge_f32 vcc, s55, v205\n" \
  "s_or_b64 s[62:63], vcc, s[50:51]\n" \
  "s_cbranch_scc1 RAREA_%=\n" \
  "RETA_%=:\n" \
  "s_waitcnt lgkmcnt(5)\n" \
  "v_mfma_f32_32x32x16_bf16 v[0:15], v[160:163], v[144:147], v[0:15]\n" \
  "ds_read_b128 v[184:187], v203 offset:7168\n" \
  "v_add_f32 v204, v204, v205\n" \
  "s_cmp_gt_i32 s47, s49\n" \
  "s_cbranch_scc1 MASKB_%=\n" \
  "MRETB_%=:\n" \
  "s_cmp_lt_u32 s70, 1\n" \
  "s_cbranch_scc1 DV0E_%=\n" \
  "s_mov_b32 m0, s46\n" \
  "s_nop 0\n" \
  "buffer_load_dwordx4 v215, s[64:67], s44 offen sc1 lds\n" \
  "buffer_load_dwordx4 v215, s[64:67], s44 offen offset:1024 sc1 lds\n" \
  "s_add_u32 s44, s44, 0x8000\n" \
  "DV0E_%=:\n" \
  "v_mfma_f32_32x32x16_bf16 v[16:31], v[164:167], v[144:147], v[16:31]\n" \
  "ds_read_b128 v[160:163], v201 offset:0\n" \
  "s_setprio 2\n" \
  "v_exp_f32 v207, v80\n" \
  "v_exp_f32 v208, v81\n" \
  "s_waitcnt lgkmcnt(5)\n" \
  "v_mfma_f32_32x32x16_bf16 v[32:47], v[188:191], v[144:147], v[32:47]\n" \
  "ds_read_b128 v[164:167], v201 offset:1024\n" \
  "v_exp_f32 v209, v82\n" \
  "v_cvt_pk_bf16_f32 v152, v207, v208\n" \
  "v_exp_f32 v210, v83\n" \
  "v_mfma_f32_32x32x16_bf16 v[48:63], v[168:171], v[144:147], v[48:63]\n" \
  "ds_read_b128 v[168:171], v201 offset:2048\n" \
  "s_cmp_lt_u32 s70, 2\n" \
  "s_cbranch_scc1 DV1E_%=\n" \
  "s_add_u32 m0, s46, 0x4000\n" \
  "s_nop 0\n" \
  "buffer_load_dwordx4 v215, s[64:67], s44 offen sc1 lds\n" \
  "buffer_load_dwordx4 v215, s[64:67], s44 offen offset:1024 sc1 lds\n" \
  "s_add_u32 s44, s44, 0x8000\n" \
  "DV1E_%=:\n" \
  "s_waitcnt lgkmcnt(5)\n" \
  "v_mfma_f32_32x32x16_bf16 v[0:15], v[172:175], v[148:151], v[0:15]\n" \
  "ds_read_b128 v[172:175], v201 offset:3072\n" \
  "s_add_u32 s47, s47, s48\n" \
  "s_sub_u32 s40, s40, 1\n" \
  "v_add_f32 v205, v207, v208\n" \
  "v_exp_f32 v211, v84\n" \
  "v_mfma_f32_32x32x16_bf16 v[16:31], v[176:179], v[148:151], v[16:31]\n" \
  "ds_read_b128 v[176:179], v201 offset:4096\n" \
  "v_swap_b32 v202, v203\n" \
  "v_cvt_pk_bf16_f32 v153, v209, v210\n" \
  "v_exp_f32 v212, v85\n" \
  "s_waitcnt lgkmcnt(5)\n" \
  "v_mfma_f32_32x32x16_bf16 v[32:47], v[180:183], v[148:151], v[32:47]\n" \
  "ds_read_b128 v[180:183], v201 offset:5120\n" \
  "s_xor_b32 s45, s45, 0x8000\n" \
  "s_xor_b32 s46, s46, 0x8000\n" \
  "v_add_f32 v206, v209, v210\n" \
  "v_exp_f32 v207, v86\n" \
  "v_mfma_f32_32x32x16_bf16 v[48:63], v[184:187], v[148:151], v[48:63]\n" \
  "ds_read_b128 v[184:187], v201 offset:6144\n" \
  "v_cvt_pk_bf16_f32 v154, v211, v212\n" \
  "v_exp_f32 v208, v87\n" \
  "v_add_f32 v205, v205, v211\n" \
  "v_swap_b32 v200, v201\n" \
  "s_cmp_lg_u32 s40, 0\n" \
  "s_cbranch_scc1 LOOP_%=\n" \
  "s_waitcnt lgkmcnt(0)\n" \
  "ds_read_b128 v[164:167], v202 offset:8192\n" \
  "ds_read_b128 v[168:171], v202 offset:10240\n" \
  "ds_read_b128 v[172:175], v202 offset:12288\n" \
  "ds_read_b128 v[176:179], v202 offset:14336\n" \
  "ds_read_b128 v[180:183], v202 offset:9216\n" \
  "ds_read_b128 v[184:187], v202 offset:11264\n" \
  "ds_read_b128 v[188:191], v202 offset:13312\n" \
  "ds_read_b128 v[160:163], v202 offset:15360\n" \
  "v_add_f32 v206, v206, v212\n" \
  "v_exp_f32 v209, v88\n" \
  "v_cvt_pk_bf16_f32 v155, v207, v208\n" \
  "v_exp_f32 v210, v89\n" \
  "v_add_f32 v205, v205, v207\n" \
  "v_add_f32 v206, v206, v208\n" \
  "v_exp_f32 v211, v90\n" \
  "v_cvt_pk_bf16_f32 v156, v209, v210\n" \
  "v_exp_f32 v212, v91\n" \
  "v_add_f32 v205, v205, v209\n" \
  "v_add_f32 v206, v206, v210\n" \
  "v_exp_f32 v207, v92\n" \
  "v_cvt_pk_bf16_f32 v157, v211, v212\n" \
  "v_exp_f32 v208, v93\n" \
  "v_add_f32 v205, v205, v211\n" \
  "v_add_f32 v206, v206, v212\n" \
  "v_exp_f32 v209, v94\n" \
  "v_cvt_pk_bf16_f32 v158, v207, v208\n" \
  "v_exp_f32 v210, v95\n" \
  "v_add_f32 v205, v205, v207\n" \
  "v_add_f32 v206, v206, v208\n" \
  "s_nop 0\n" \
  "v_cvt_pk_bf16_f32 v159, v209, v210\n" \
  "v_add_f32 v205, v205, v209\n" \
  "v_add_f32 v206, v206, v210\n" \
  "v_add_f32 v205, v205, v206\n" \
  "v_cmp_nge_f32 vcc, s55, v205\n" \
  "s_or_b64 s[62:63], vcc, s[52:53]\n" \
  "s_cbranch_scc1 RARET_%=\n" \
  "RETT_%=:\n" \
  "v_add_f32 v204, v204, v205\n" \
  "s_waitcnt lgkmcnt(7)\n" \
  "v_mfma_f32_32x32x16_bf16 v[0:15], v[164:167], v[152:155], v[0:15]\n" \
  "s_waitcnt lgkmcnt(6)\n" \
  "v_mfma_f32_32x32x16_bf16 v[16:31], v[168:171], v[152:155], v[16:31]\n" \
  "s_waitcnt lgkmcnt(5)\n" \
  "v_mfma_f32_32x32x16_bf16 v[32:47], v[172:175], v[152:155], v[32:47]\n" \
  "s_waitcnt lgkmcnt(4)\n" \
  "v_mfma_f32_32x32x16_bf16 v[48:63], v[176:179], v[152:155], v[48:63]\n" \
  "s_waitcnt lgkmcnt(3)\n" \
  "v_mfma_f32_32x32x16_bf16 v[0:15], v[180:183], v[156:159], v[0:15]\n" \
  "s_waitcnt lgkmcnt(2)\n" \
  "v_mfma_f32_32x32x16_bf16 v[16:31], v[184:187], v[156:159], v[16:31]\n" \
  "s_waitcnt lgkmcnt(1)\n" \
  "v_mfma_f32_32x32x16_bf16 v[32:47], v[188:191], v[156:159], v[32:47]\n" \
  "s_waitcnt lgkmcnt(0)\n" \
  "v_mfma_f32_32x32x16_bf16 v[48:63], v[160:163], v[156:159], v[48:63]\n" \
  "s_nop 15\n" \
  "s_branch FIN_%=\n" \
  "RAREA_%=:\n" \
  "s_nop 7\n" \
  "v_max3_f32 v216, v64, v65, v66\n" \
  "v_max3_f32 v217, v67, v68, v69\n" \
  "v_max3_f32 v216, v216, v70, v71\n" \
  "v_max3_f32 v217, v217, v72, v73\n" \
  "v_max3_f32 v216, v216, v74, v75\n" \
  "v_max3_f32 v217, v217, v76, v77\n" \
  "v_max3_f32 v216, v216, v78, v79\n" \
  "v_max_f32 v216, v216, v217\n" \
  "v_mov_b32 v217, v216\n" \
  "s_nop 1\n" \
  "v_permlane32_swap_b32 v216, v217\n" \
  "s_nop 1\n" \
  "v_max_f32 v216, v216, v217\n" \
  "v_sub_f32 v216, v216, v213\n" \
  "v_cmp_lt_f32 vcc, s54, v216\n" \
  "s_nop 1\n" \
  "v_cndmask_b32 v216, 0, v216, vcc\n" \
  "v_max_f32 v217, 0, v216\n" \
  "v_exp_f32 v217, -v217\n" \
  "v_add_f32 v218, v213, v216\n" \
  "v_sub_f32 v64, v64, v218\n" \
  "v_sub_f32 v65, v65, v218\n" \
  "v_sub_f32 v66, v66, v218\n" \
  "v_sub_f32 v67, v67, v218\n" \
  "v_sub_f32 v68, v68, v218\n" \
  "v_sub_f32 v69, v69, v218\n" \
  "v_sub_f32 v70, v70, v218\n" \
  "v_sub_f32 v71, v71, v218\n" \
  "v_sub_f32 v72, v72, v218\n" \
  "v_sub_f32 v73, v73, v218\n" \
  "v_sub_f32 v74, v74, v218\n" \
  "v_sub_f32 v75, v75, v218\n" \
  "v_sub_f32 v76, v76, v218\n" \
  "v_sub_f32 v77, v77, v218\n" \
  "v_sub_f32 v78, v78, v218\n" \
  "v_sub_f32 v79, v79, v218\n" \
  "v_sub_f32 v96, v96, v216\n" \
  "v_mov_b32 v97, v96\n" \
  "v_mov_b32 v98, v96\n" \
  "v_mov_b32 v99, v96\n" \
  "v_mov_b32 v100, v96\n" \
  "v_mov_b32 v101, v96\n" \
  "v_mov_b32 v102, v96\n" \
  "v_mov_b32 v103, v96\n" \
  "v_mov_b32 v104, v96\n" \
  "v_mov_b32 v105, v96\n" \
  "v_mov_b32 v106, v96\n" \
  "v_mov_b32 v107, v96\n" \
  "v_mov_b32 v108, v96\n" \
  "v_mov_b32 v109, v96\n" \
  "v_mov_b32 v110, v96\n" \
  "v_mov_b32 v111, v96\n" \
  "v_mul_f32 v0, v0, v217\n" \
  "v_mul_f32 v1, v1, v217\n" \
  "v_mul_f32 v2, v2, v217\n" \
  "v_mul_f32 v3, v3, v217\n" \
  "v_mul_f32 v4, v4, v217\n" \
  "v_mul_f32 v5, v5, v217\n" \
  "v_mul_f32 v6, v6, v217\n" \
  "v_mul_f32 v7, v7, v217\n" \
  "v_mul_f32 v8, v8, v217\n" \
  "v_mul_f32 v9, v9, v217\n" \
  "v_mul_f32 v10, v10, v217\n" \
  "v_mul_f32 v11, v11, v217\n" \
  "v_mul_f32 v12, v12, v217\n" \
  "v_mul_f32 v13, v13, v217\n" \
  "v_mul_f32 v14, v14, v217\n" \
  "v_mul_f32 v15, v15, v217\n" \
  "v_mul_f32 v16, v16, v217\n" \
  "v_mul_f32 v17, v17, v217\n" \
  "v_mul_f32 v18, v18, v217\n" \
  "v_mul_f32 v19, v19, v217\n" \
  "v_mul_f32 v20, v20, v217\n" \
  "v_mul_f32 v21, v21, v217\n" \
  "v_mul_f32 v22, v22, v217\n" \
  "v_mul_f32 v23, v23, v217\n" \
  "v_mul_f32 v24, v24, v217\n" \
  "v_mul_f32 v25, v25, v217\n" \
  "v_mul_f32 v26, v26, v217\n" \
  "v_mul_f32 v27, v27, v217\n" \
  "v_mul_f32 v28, v28, v217\n" \
  "v_mul_f32 v29, v29, v217\n" \
  "v_mul_f32 v30, v30, v217\n" \
  "v_mul_f32 v31, v31, v217\n" \
  "v_mul_f32 v32, v32, v217\n" \
  "v_mul_f32 v33, v33, v217\n" \
  "v_mul_f32 v34, v34, v217\n" \
  "v_mul_f32 v35, v35, v217\n" \
  "v_mul_f32 v36, v36, v217\n" \
  "v_mul_f32 v37, v37, v217\n" \
  "v_mul_f32 v38, v38, v217\n" \
  "v_mul_f32 v39, v39, v217\n" \
  "v_mul_f32 v40, v40, v217\n" \
  "v_mul_f32 v41, v41, v217\n" \
  "v_mul_f32 v42, v42, v217\n" \
  "v_mul_f32 v43, v43, v217\n" \
  "v_mul_f32 v44, v44, v217\n" \
  "v_mul_f32 v45, v45, v217\n" \
  "v_mul_f32 v46, v46, v217\n" \
  "v_mul_f32 v47, v47, v217\n" \
  "v_mul_f32 v48, v48, v217\n" \
  "v_mul_f32 v49, v49, v217\n" \
  "v_mul_f32 v50, v50, v217\n" \
  "v_mul_f32 v51, v51, v217\n" \
  "v_mul_f32 v52, v52, v217\n" \
  "v_mul_f32 v53, v53, v217\n" \
  "v_mul_f32 v54, v54, v217\n" \
  "v_mul_f32 v55, v55, v217\n" \
  "v_mul_f32 v56, v56, v217\n" \
  "v_mul_f32 v57, v57, v217\n" \
  "v_mul_f32 v58, v58, v217\n" \
  "v_mul_f32 v59, v59, v217\n" \
  "v_mul_f32 v60, v60, v217\n" \
  "v_mul_f32 v61, v61, v217\n" \
  "v_mul_f32 v62, v62, v217\n" \
  "v_mul_f32 v63, v63, v217\n" \
  "v_mul_f32 v204, v204, v217\n" \
  "v_exp_f32 v207, v64\n" \
  "v_exp_f32 v208, v65\n" \
  "v_exp_f32 v209, v66\n" \
  "v_cvt_pk_bf16_f32 v144, v207, v208\n" \
  "v_exp_f32 v210, v67\n" \
  "v_add_f32 v205, v207, v208\n" \
  "v_exp_f32 v211, v68\n" \
  "v_cvt_pk_bf16_f32 v145, v209, v210\n" \
  "v_exp_f32 v212, v69\n" \
  "v_add_f32 v206, v209, v210\n" \
  "v_exp_f32 v207, v70\n" \
  "v_cvt_pk_bf16_f32 v146, v211, v212\n" \
  "v_exp_f32 v208, v71\n" \
  "v_add_f32 v205, v205, v211\n" \
  "v_add_f32 v206, v206, v212\n" \
  "v_exp_f32 v209, v72\n" \
  "v_cvt_pk_bf16_f32 v147, v207, v208\n" \
  "v_exp_f32 v210, v73\n" \
  "v_add_f32 v205, v205, v207\n" \
  "v_add_f32 v206, v206, v208\n" \
  "v_exp_f32 v211, v74\n" \
  "v_cvt_pk_bf16_f32 v148, v209, v210\n" \
  "v_exp_f32 v212, v75\n" \
  "v_add_f32 v205, v205, v209\n" \
  "v_add_f32 v206, v206, v210\n" \
  "v_exp_f32 v207, v76\n" \
  "v_cvt_pk_bf16_f32 v149, v211, v212\n" \
  "v_exp_f32 v208, v77\n" \
  "v_add_f32 v205, v205, v211\n" \
  "v_add_f32 v206, v206, v212\n" \
  "v_exp_f32 v209, v78\n" \
  "v_cvt_pk_bf16_f32 v150, v207, v208\n" \
  "v_exp_f32 v210, v79\n" \
  "v_add_f32 v205, v205, v207\n" \
  "v_add_f32 v206, v206, v208\n" \
  "s_nop 0\n" \
  "v_cvt_pk_bf16_f32 v151, v209, v210\n" \
  "v_add_f32 v205, v205, v209\n" \
  "v_add_f32 v206, v206, v210\n" \
  "v_add_f32 v205, v205, v206\n" \
  "v_mov_b32 v213, v216\n" \
  "v_cmp_neq_f32 s[52:53], 0, v216\n" \
  "s_mov_b64 s[50:51], 0\n" \
  "s_mov_b32 s54, 4.0\n" \
  "s_nop 1\n" \
  "s_branch RETA_%=\n" \
  "RAREB_%=:\n" \
  "s_nop 7\n" \
  "v_max3_f32 v216, v80, v81, v82\n" \
  "v_max3_f32 v217, v83, v84, v85\n" \
  "v_max3_f32 v216, v216, v86, v87\n" \
  "v_max3_f32 v217, v217, v88, v89\n" \
  "v_max3_f32 v216, v216, v90, v91\n" \
  "v_max3_f32 v217, v217, v92, v93\n" \
  "v_max3_f32 v216, v216, v94, v95\n" \
  "v_max_f32 v216, v216, v217\n" \
  "v_mov_b32 v217, v216\n" \
  "s_nop 1\n" \
  "v_permlane32_swap_b32 v216, v217\n" \
  "s_nop 1\n" \
  "v_max_f32 v216, v216, v217\n" \
  "v_sub_f32 v216, v216, v213\n" \
  "v_cmp_lt_f32 vcc, s54, v216\n" \
  "s_nop 1\n" \
  "v_cndmask_b32 v216, 0, v216, vcc\n" \
  "v_max_f32 v217, 0, v216\n" \
  "v_exp_f32 v217, -v217\n" \
  "v_add_f32 v218, v213, v216\n" \
  "v_sub_f32 v80, v80, v218\n" \
  "v_sub_f32 v81, v81, v218\n" \
  "v_sub_f32 v82, v82, v218\n" \
  "v_sub_f32 v83, v83, v218\n" \
  "v_sub_f32 v84, v84, v218\n" \
  "v_sub_f32 v85, v85, v218\n" \
  "v_sub_f32 v86, v86, v218\n" \
  "v_sub_f32 v87, v87, v218\n" \
  "v_sub_f32 v88, v88, v218\n" \
  "v_sub_f32 v89, v89, v218\n" \
  "v_sub_f32 v90, v90, v218\n" \
  "v_sub_f32 v91, v91, v218\n" \
  "v_sub_f32 v92, v92, v218\n" \
  "v_sub_f32 v93, v93, v218\n" \
  "v_sub_f32 v94, v94, v218\n" \
  "v_sub_f32 v95, v95, v218\n" \
  "v_sub_f32 v96, v96, v216\n" \
  "v_mov_b32 v97, v96\n" \
  "v_mov_b32 v98, v96\n" \
  "v_mov_b32 v99, v96\n" \
  "v_mov_b32 v100, v96\n" \
  "v_mov_b32 v101, v96\n" \
  "v_mov_b32 v102, v96\n" \
  "v_mov_b32 v103, v96\n" \
  "v_mov_b32 v104, v96\n" \
  "v_mov_b32 v105, v96\n" \
  "v_mov_b32 v106, v96\n" \
  "v_mov_b32 v107, v96\n" \
  "v_mov_b32 v108, v96\n" \
  "v_mov_b32 v109, v96\n" \
  "v_mov_b32 v110, v96\n" \
  "v_mov_b32 v111, v96\n" \
  "v_mul_f32 v0, v0, v217\n" \
  "v_mul_f32 v1, v1, v217\n" \
  "v_mul_f32 v2, v2, v217\n" \
  "v_mul_f32 v3, v3, v217\n" \
  "v_mul_f32 v4, v4, v217\n" \
  "v_mul_f32 v5, v5, v217\n" \
  "v_mul_f32 v6, v6, v217\n" \
  "v_mul_f32 v7, v7, v217\n" \
  "v_mul_f32 v8, v8, v217\n" \
  "v_mul_f32 v9, v9, v217\n" \
  "v_mul_f32 v10, v10, v217\n" \
  "v_mul_f32 v11, v11, v217\n" \
  "v_mul_f32 v12, v12, v217\n" \
  "v_mul_f32 v13, v13, v217\n" \
  "v_mul_f32 v14, v14, v217\n" \
  "v_mul_f32 v15, v15, v217\n" \
  "v_mul_f32 v16, v16, v217\n" \
  "v_mul_f32 v17, v17, v217\n" \
  "v_mul_f32 v18, v18, v217\n" \
  "v_mul_f32 v19, v19, v217\n" \
  "v_mul_f32 v20, v20, v217\n" \
  "v_mul_f32 v21, v21, v217\n" \
  "v_mul_f32 v22, v22, v217\n" \
  "v_mul_f32 v23, v23, v217\n" \
  "v_mul_f32 v24, v24, v217\n" \
  "v_mul_f32 v25, v25, v217\n" \
  "v_mul_f32 v26, v26, v217\n" \
  "v_mul_f32 v27, v27, v217\n" \
  "v_mul_f32 v28, v28, v217\n" \
  "v_mul_f32 v29, v29, v217\n" \
  "v_mul_f32 v30, v30, v217\n" \
  "v_mul_f32 v31, v31, v217\n" \
  "v_mul_f32 v32, v32, v217\n" \
  "v_mul_f32 v33, v33, v217\n" \
  "v_mul_f32 v34, v34, v217\n" \
  "v_mul_f32 v35, v35, v217\n" \
  "v_mul_f32 v36, v36, v217\n" \
  "v_mul_f32 v37, v37, v217\n" \
  "v_mul_f32 v38, v38, v217\n" \
  "v_mul_f32 v39, v39, v217\n" \
  "v_mul_f32 v40, v40, v217\n" \
  "v_mul_f32 v41, v41, v217\n" \
  "v_mul_f32 v42, v42, v217\n" \
  "v_mul_f32 v43, v43, v217\n" \
  "v_mul_f32 v44, v44, v217\n" \
  "v_mul_f32 v45, v45, v217\n" \
  "v_mul_f32 v46, v46, v217\n" \
  "v_mul_f32 v47, v47, v217\n" \
  "v_mul_f32 v48, v48, v217\n" \
  "v_mul_f32 v49, v49, v217\n" \
  "v_mul_f32 v50, v50, v217\n" \
  "v_mul_f32 v51, v51, v217\n" \
  "v_mul_f32 v52, v52, v217\n" \
  "v_mul_f32 v53, v53, v217\n" \
  "v_mul_f32 v54, v54, v217\n" \
  "v_mul_f32 v55, v55, v217\n" \
  "v_mul_f32 v56, v56, v217\n" \
  "v_mul_f32 v57, v57, v217\n" \
  "v_mul_f32 v58, v58, v217\n" \
  "v_mul_f32 v59, v59, v217\n" \
  "v_mul_f32 v60, v60, v217\n" \
  "v_mul_f32 v61, v61, v217\n" \
  "v_mul_f32 v62, v62, v217\n" \
  "v_mul_f32 v63, v63, v217\n" \
  "v_mul_f32 v204, v204, v217\n" \
  "v_exp_f32 v207, v80\n" \
  "v_exp_f32 v208, v81\n" \
  "v_exp_f32 v209, v82\n" \
  "v_cvt_pk_bf16_f32 v152, v207, v208\n" \
  "v_exp_f32 v210, v83\n" \
  "v_add_f32 v205, v207, v208\n" \
  "v_exp_f32 v211, v84\n" \
  "v_cvt_pk_bf16_f32 v153, v209, v210\n" \
  "v_exp_f32 v212, v85\n" \
  "v_add_f32 v206, v209, v210\n" \
  "v_exp_f32 v207, v86\n" \
  "v_cvt_pk_bf16_f32 v154, v211, v212\n" \
  "v_exp_f32 v208, v87\n" \
  "v_add_f32 v205, v205, v211\n" \
  "v_add_f32 v206, v206, v212\n" \
  "v_exp_f32 v209, v88\n" \
  "v_cvt_pk_bf16_f32 v155, v207, v208\n" \
  "v_exp_f32 v210, v89\n" \
  "v_add_f32 v205, v205, v207\n" \
  "v_add_f32 v206, v206, v208\n" \
  "v_exp_f32 v211, v90\n" \
  "v_cvt_pk_bf16_f32 v156, v209, v210\n" \
  "v_exp_f32 v212, v91\n" \
  "v_add_f32 v205, v205, v209\n" \
  "v_add_f32 v206, v206, v210\n" \
  "v_exp_f32 v207, v92\n" \
  "v_cvt_pk_bf16_f32 v157, v211, v212\n" \
  "v_exp_f32 v208, v93\n" \
  "v_add_f32 v205, v205, v211\n" \
  "v_add_f32 v206, v206, v212\n" \
  "v_exp_f32 v209, v94\n" \
  "v_cvt_pk_bf16_f32 v158, v207, v208\n" \
  "v_exp_f32 v210, v95\n" \
  "v_add_f32 v205, v205, v207\n" \
  "v_add_f32 v206, v206, v208\n" \
  "s_nop 0\n" \
  "v_cvt_pk_bf16_f32 v159, v209, v210\n" \
  "v_add_f32 v205, v205, v209\n" \
  "v_add_f32 v206, v206, v210\n" \
  "v_add_f32 v205, v205, v206\n" \
  "v_mov_b32 v213, v216\n" \
  "v_cmp_neq_f32 s[50:51], 0, v216\n" \
  "s_mov_b64 s[52:53], 0\n" \
  "s_mov_b32 s54, 4.0\n" \
  "s_nop 1\n" \
  "s_branch RETB_%=\n" \
  "RARET_%=:\n" \
  "s_nop 7\n" \
  "v_max3_f32 v216, v80, v81, v82\n" \
  "v_max3_f32 v217, v83, v84, v85\n" \
  "v_max3_f32 v216, v216, v86, v87\n" \
  "v_max3_f32 v217, v217, v88, v89\n" \
  "v_max3_f32 v216, v216, v90, v91\n" \
  "v_max3_f32 v217, v217, v92, v93\n" \
  "v_max3_f32 v216, v216, v94, v95\n" \
  "v_max_f32 v216, v216, v217\n" \
  "v_mov_b32 v217, v216\n" \
  "s_nop 1\n" \
  "v_permlane32_swap_b32 v216, v217\n" \
  "s_nop 1\n" \
  "v_max_f32 v216, v216, v217\n" \
  "v_sub_f32 v216, v216, v213\n" \
  "v_cmp_lt_f32 vcc, s54, v216\n" \
  "s_nop 1\n" \
  "v_cndmask_b32 v216, 0, v216, vcc\n" \
  "v_max_f32 v217, 0, v216\n" \
  "v_exp_f32 v217, -v217\n" \
  "v_add_f32 v218, v213, v216\n" \
  "v_sub_f32 v80, v80, v218\n" \
  "v_sub_f32 v81, v81, v218\n" \
  "v_sub_f32 v82, v82, v218\n" \
  "v_sub_f32 v83, v83, v218\n" \
  "v_sub_f32 v84, v84, v218\n" \
  "v_sub_f32 v85, v85, v218\n" \
  "v_sub_f32 v86, v86, v218\n" \
  "v_sub_f32 v87, v87, v218\n" \
  "v_sub_f32 v88, v88, v218\n" \
  "v_sub_f32 v89, v89, v218\n" \
  "v_sub_f32 v90, v90, v218\n" \
  "v_sub_f32 v91, v91, v218\n" \
  "v_sub_f32 v92, v92, v218\n" \
  "v_sub_f32 v93, v93, v218\n" \
  "v_sub_f32 v94, v94, v218\n" \
  "v_sub_f32 v95, v95, v218\n" \
  "v_sub_f32 v96, v96, v216\n" \
  "v_mov_b32 v97, v96\n" \
  "v_mov_b32 v98, v96\n" \
  "v_mov_b32 v99, v96\n" \
  "v_mov_b32 v100, v96\n" \
  "v_mov_b32 v101, v96\n" \
  "v_mov_b32 v102, v96\n" \
  "v_mov_b32 v103, v96\n" \
  "v_mov_b32 v104, v96\n" \
  "v_mov_b32 v105, v96\n" \
  "v_mov_b32 v106, v96\n" \
  "v_mov_b32 v107, v96\n" \
  "v_mov_b32 v108, v96\n" \
  "v_mov_b32 v109, v96\n" \
  "v_mov_b32 v110, v96\n" \
  "v_mov_b32 v111, v96\n" \
  "v_mul_f32 v0, v0, v217\n" \
  "v_mul_f32 v1, v1, v217\n" \
  "v_mul_f32 v2, v2, v217\n" \
  "v_mul_f32 v3, v3, v217\n" \
  "v_mul_f32 v4, v4, v217\n" \
  "v_mul_f32 v5, v5, v217\n" \
  "v_mul_f32 v6, v6, v217\n" \
  "v_mul_f32 v7, v7, v217\n" \
  "v_mul_f32 v8, v8, v217\n" \
  "v_mul_f32 v9, v9, v217\n" \
  "v_mul_f32 v10, v10, v217\n" \
  "v_mul_f32 v11, v11, v217\n" \
  "v_mul_f32 v12, v12, v217\n" \
  "v_mul_f32 v13, v13, v217\n" \
  "v_mul_f32 v14, v14, v217\n" \
  "v_mul_f32 v15, v15, v217\n" \
  "v_mul_f32 v16, v16, v217\n" \
  "v_mul_f32 v17, v17, v217\n" \
  "v_mul_f32 v18, v18, v217\n" \
  "v_mul_f32 v19, v19, v217\n" \
  "v_mul_f32 v20, v20, v217\n" \
  "v_mul_f32 v21, v21, v217\n" \
  "v_mul_f32 v22, v22, v217\n" \
  "v_mul_f32 v23, v23, v217\n" \
  "v_mul_f32 v24, v24, v217\n" \
  "v_mul_f32 v25, v25, v217\n" \
  "v_mul_f32 v26, v26, v217\n" \
  "v_mul_f32 v27, v27, v217\n" \
  "v_mul_f32 v28, v28, v217\n" \
  "v_mul_f32 v29, v29, v217\n" \
  "v_mul_f32 v30, v30, v217\n" \
  "v_mul_f32 v31, v31, v217\n" \
  "v_mul_f32 v32, v32, v217\n" \
  "v_mul_f32 v33, v33, v217\n" \
  "v_mul_f32 v34, v34, v217\n" \
  "v_mul_f32 v35, v35, v217\n" \
  "v_mul_f32 v36, v36, v217\n" \
  "v_mul_f32 v37, v37, v217\n" \
  "v_mul_f32 v38, v38, v217\n" \
  "v_mul_f32 v39, v39, v217\n" \
  "v_mul_f32 v40, v40, v217\n" \
  "v_mul_f32 v41, v41, v217\n" \
  "v_mul_f32 v42, v42, v217\n" \
  "v_mul_f32 v43, v43, v217\n" \
  "v_mul_f32 v44, v44, v217\n" \
  "v_mul_f32 v45, v45, v217\n" \
  "v_mul_f32 v46, v46, v217\n" \
  "v_mul_f32 v47, v47, v217\n" \
  "v_mul_f32 v48, v48, v217\n" \
  "v_mul_f32 v49, v49, v217\n" \
  "v_mul_f32 v50, v50, v217\n" \
  "v_mul_f32 v51, v51, v217\n" \
  "v_mul_f32 v52, v52, v217\n" \
  "v_mul_f32 v53, v53, v217\n" \
  "v_mul_f32 v54, v54, v217\n" \
  "v_mul_f32 v55, v55, v217\n" \
  "v_mul_f32 v56, v56, v217\n" \
  "v_mul_f32 v57, v57, v217\n" \
  "v_mul_f32 v58, v58, v217\n" \
  "v_mul_f32 v59, v59, v217\n" \
  "v_mul_f32 v60, v60, v217\n" \
  "v_mul_f32 v61, v61, v217\n" \
  "v_mul_f32 v62, v62, v217\n" \
  "v_mul_f32 v63, v63, v217\n" \
  "v_mul_f32 v204, v204, v217\n" \
  "v_exp_f32 v207, v80\n" \
  "v_exp_f32 v208, v81\n" \
  "v_exp_f32 v209, v82\n" \
  "v_cvt_pk_bf16_f32 v152, v207, v208\n" \
  "v_exp_f32 v210, v83\n" \
  "v_add_f32 v205, v207, v208\n" \
  "v_exp_f32 v211, v84\n" \
  "v_cvt_pk_bf16_f32 v153, v209, v210\n" \
  "v_exp_f32 v212, v85\n" \
  "v_add_f32 v206, v209, v210\n" \
  "v_exp_f32 v207, v86\n" \
  "v_cvt_pk_bf16_f32 v154, v211, v212\n" \
  "v_exp_f32 v208, v87\n" \
  "v_add_f32 v205, v205, v211\n" \
  "v_add_f32 v206, v206, v212\n" \
  "v_exp_f32 v209, v88\n" \
  "v_cvt_pk_bf16_f32 v155, v207, v208\n" \
  "v_exp_f32 v210, v89\n" \
  "v_add_f32 v205, v205, v207\n" \
  "v_add_f32 v206, v206, v208\n" \
  "v_exp_f32 v211, v90\n" \
  "v_cvt_pk_bf16_f32 v156, v209, v210\n" \
  "v_exp_f32 v212, v91\n" \
  "v_add_f32 v205, v205, v209\n" \
  "v_add_f32 v206, v206, v210\n" \
  "v_exp_f32 v207, v92\n" \
  "v_cvt_pk_bf16_f32 v157, v211, v212\n" \
  "v_exp_f32 v208, v93\n" \
  "v_add_f32 v205, v205, v211\n" \
  "v_add_f32 v206, v206, v212\n" \
  "v_exp_f32 v209, v94\n" \
  "v_cvt_pk_bf16_f32 v158, v207, v208\n" \
  "v_exp_f32 v210, v95\n" \
  "v_add_f32 v205, v205, v207\n" \
  "v_add_f32 v206, v206, v208\n" \
  "s_nop 0\n" \
  "v_cvt_pk_bf16_f32 v159, v209, v210\n" \
  "v_add_f32 v205, v205, v209\n" \
  "v_add_f32 v206, v206, v210\n" \
  "v_add_f32 v205, v205, v206\n" \
  "v_mov_b32 v213, v216\n" \
  "v_cmp_neq_f32 s[50:51], 0, v216\n" \
  "s_mov_b64 s[52:53], 0\n" \
  "s_mov_b32 s54, 4.0\n" \
  "s_nop 1\n" \
  "s_branch RETT_%=\n" \
  "MASKA_%=:\n" \
  "s_nop 7\n" \
  "s_add_u32 s68, s47, 0\n" \
  "v_subrev_u32 v216, s68, v214\n" \
  "v_cmp_gt_i32 s[56:57], 0, v216\n" \
  "v_cmp_gt_i32 s[58:59], 1, v216\n" \
  "v_cmp_gt_i32 s[60:61], 2, v216\n" \
  "v_cndmask_b32 v64, v64, v220, s[56:57]\n" \
  "v_cmp_gt_i32 s[56:57], 3, v216\n" \
  "v_cndmask_b32 v65, v65, v220, s[58:59]\n" \
  "v_cmp_gt_i32 s[58:59], 8, v216\n" \
  "v_cndmask_b32 v66, v66, v220, s[60:61]\n" \
  "v_cmp_gt_i32 s[60:61], 9, v216\n" \
  "v_cndmask_b32 v67, v67, v220, s[56:57]\n" \
  "v_cmp_gt_i32 s[56:57], 10, v216\n" \
  "v_cndmask_b32 v68, v68, v220, s[58:59]\n" \
  "v_cmp_gt_i32 s[58:59], 11, v216\n" \
  "v_cndmask_b32 v69, v69, v220, s[60:61]\n" \
  "v_cmp_gt_i32 s[60:61], 16, v216\n" \
  "v_cndmask_b32 v70, v70, v220, s[56:57]\n" \
  "v_cmp_gt_i32 s[56:57], 17, v216\n" \
  "v_cndmask_b32 v71, v71, v220, s[58:59]\n" \
  "v_cmp_gt_i32 s[58:59], 18, v216\n" \
  "v_cndmask_b32 v72, v72, v220, s[60:61]\n" \
  "v_cmp_gt_i32 s[60:61], 19, v216\n" \
  "v_cndmask_b32 v73, v73, v220, s[56:57]\n" \
  "v_cmp_gt_i32 s[56:57], 24, v216\n" \
  "v_cndmask_b32 v74, v74, v220, s[58:59]\n" \
  "v_cmp_gt_i32 s[58:59], 25, v216\n" \
  "v_cndmask_b32 v75, v75, v220, s[60:61]\n" \
  "v_cmp_gt_i32 s[60:61], 26, v216\n" \
  "v_cndmask_b32 v76, v76, v220, s[56:57]\n" \
  "v_cmp_gt_i32 s[56:57], 27, v216\n" \
  "v_cndmask_b32 v77, v77, v220, s[58:59]\n" \
  "s_nop 0\n" \
  "v_cndmask_b32 v78, v78, v220, s[60:61]\n" \
  "s_nop 0\n" \
  "v_cndmask_b32 v79, v79, v220, s[56:57]\n" \
  "s_branch MRETA_%=\n" \
  "MASKB_%=:\n" \
  "s_nop 7\n" \
  "s_add_u32 s68, s47, 32\n" \
  "v_subrev_u32 v216, s68, v214\n" \
  "v_cmp_gt_i32 s[56:57], 0, v216\n" \
  "v_cmp_gt_i32 s[58:59], 1, v216\n" \
  "v_cmp_gt_i32 s[60:61], 2, v216\n" \
  "v_cndmask_b32 v80, v80, v220, s[56:57]\n" \
  "v_cmp_gt_i32 s[56:57], 3, v216\n" \
  "v_cndmask_b32 v81, v81, v220, s[58:59]\n" \
  "v_cmp_gt_i32 s[58:59], 8, v216\n" \
  "v_cndmask_b32 v82, v82, v220, s[60:61]\n" \
  "v_cmp_gt_i32 s[60:61], 9, v216\n" \
  "v_cndmask_b32 v83, v83, v220, s[56:57]\n" \
  "v_cmp_gt_i32 s[56:57], 10, v216\n" \
  "v_cndmask_b32 v84, v84, v220, s[58:59]\n" \
  "v_cmp_gt_i32 s[58:59], 11, v216\n" \
  "v_cndmask_b32 v85, v85, v220, s[60:61]\n" \
  "v_cmp_gt_i32 s[60:61], 16, v216\n" \
  "v_cndmask_b32 v86, v86, v220, s[56:57]\n" \
  "v_cmp_gt_i32 s[56:57], 17, v216\n" \
  "v_cndmask_b32 v87, v87, v220, s[58:59]\n" \
  "v_cmp_gt_i32 s[58:59], 18, v216\n" \
  "v_cndmask_b32 v88, v88, v220, s[60:61]\n" \
  "v_cmp_gt_i32 s[60:61], 19, v216\n" \
  "v_cndmask_b32 v89, v89, v220, s[56:57]\n" \
  "v_cmp_gt_i32 s[56:57], 24, v216\n" \
  "v_cndmask_b32 v90, v90, v220, s[58:59]\n" \
  "v_cmp_gt_i32 s[58:59], 25, v216\n" \
  "v_cndmask_b32 v91, v91, v220, s[60:61]\n" \
  "v_cmp_gt_i32 s[60:61], 26, v216\n" \
  "v_cndmask_b32 v92, v92, v220, s[56:57]\n" \
  "v_cmp_gt_i32 s[56:57], 27, v216\n" \
  "v_cndmask_b32 v93, v93, v220, s[58:59]\n" \
  "s_nop 0\n" \
  "v_cndmask_b32 v94, v94, v220, s[60:61]\n" \
  "s_nop 0\n" \
  "v_cndmask_b32 v95, v95, v220, s[56:57]\n" \
  "s_branch MRETB_%=\n" \
  "MASKE_%=:\n" \
  "s_nop 7\n" \
  "s_add_u32 s68, s47, 0\n" \
  "v_subrev_u32 v216, s68, v214\n" \
  "v_cmp_gt_i32 s[56:57], 0, v216\n" \
  "v_cmp_gt_i32 s[58:59], 1, v216\n" \
  "v_cmp_gt_i32 s[60:61], 2, v216\n" \
  "v_cndmask_b32 v64, v64, v220, s[56:57]\n" \
  "v_cmp_gt_i32 s[56:57], 3, v216\n" \
  "v_cndmask_b32 v65, v65, v220, s[58:59]\n" \
  "v_cmp_gt_i32 s[58:59], 8, v216\n" \
  "v_cndmask_b32 v66, v66, v220, s[60:61]\n" \
  "v_cmp_gt_i32 s[60:61], 9, v216\n" \
  "v_cndmask_b32 v67, v67, v220, s[56:57]\n" \
  "v_cmp_gt_i32 s[56:57], 10, v216\n" \
  "v_cndmask_b32 v68, v68, v220, s[58:59]\n" \
  "v_cmp_gt_i32 s[58:59], 11, v216\n" \
  "v_cndmask_b32 v69, v69, v220, s[60:61]\n" \
  "v_cmp_gt_i32 s[60:61], 16, v216\n" \
  "v_cndmask_b32 v70, v70, v220, s[56:57]\n" \
  "v_cmp_gt_i32 s[56:57], 17, v216\n" \
  "v_cndmask_b32 v71, v71, v220, s[58:59]\n" \
  "v_cmp_gt_i32 s[58:59], 18, v216\n" \
  "v_cndmask_b32 v72, v72, v220, s[60:61]\n" \
  "v_cmp_gt_i32 s[60:61], 19, v216\n" \
  "v_cndmask_b32 v73, v73, v220, s[56:57]\n" \
  "v_cmp_gt_i32 s[56:57], 24, v216\n" \
  "v_cndmask_b32 v74, v74, v220, s[58:59]\n" \
  "v_cmp_gt_i32 s[58:59], 25, v216\n" \
  "v_cndmask_b32 v75, v75, v220, s[60:61]\n" \
  "v_cmp_gt_i32 s[60:61], 26, v216\n" \
  "v_cndmask_b32 v76, v76, v220, s[56:57]\n" \
  "v_cmp_gt_i32 s[56:57], 27, v216\n" \
  "v_cndmask_b32 v77, v77, v220, s[58:59]\n" \
  "s_nop 0\n" \
  "v_cndmask_b32 v78, v78, v220, s[60:61]\n" \
  "s_nop 0\n" \
  "v_cndmask_b32 v79, v79, v220, s[56:57]\n" \
  "s_branch MRETE_%=\n" \
  "WRM_%=:\n" \
  "s_sub_u32 s87, s87, 1\n" \
  "s_cmp_eq_u32 s40, 1\n" \
  "s_cbranch_scc1 WRMRET_%=\n" \
  "s_mov_b32 s87, 99\n" \
  "s_mov_b32 s68, s76\n" \
  "buffer_load_dword v221, v219, s[72:75], s68 offen\n" \
  "s_add_u32 s68, s68, s77\n" \
  "buffer_load_dword v221, v219, s[72:75], s68 offen\n" \
  "s_add_u32 s68, s68, s77\n" \
  "buffer_load_dword v221, v219, s[72:75], s68 offen\n" \
  "s_add_u32 s68, s68, s77\n" \
  "buffer_load_dword v221, v219, s[72:75], s68 offen\n" \
  "s_add_u32 s68, s68, s77\n" \
  "s_branch WRMRET_%=\n" \
  "RL_%=:\n" \
  "s_mov_b32 m0, s78\n" \
  "s_add_u32 s78, s78, 0x400\n" \
  "buffer_load_dwordx4 v219, s[72:75], s76 offen lds\n" \
  "s_add_u32 s76, s76, s77\n" \
  "s_mov_b32 m0, s78\n" \
  "s_add_u32 s78, s78, 0x400\n" \
  "buffer_load_dwordx4 v219, s[72:75], s76 offen lds\n" \
  "s_add_u32 s76, s76, s77\n" \
  "s_mov_b32 m0, s78\n" \
  "s_add_u32 s78, s78, 0x400\n" \
  "buffer_load_dwordx4 v219, s[72:75], s76 offen lds\n" \
  "s_add_u32 s76, s76, s77\n" \
  "s_mov_b32 m0, s78\n" \
  "s_add_u32 s78, s78, 0x400\n" \
  "buffer_load_dwordx4 v219, s[72:75], s76 offen lds\n" \
  "s_add_u32 s76, s76, s77\n" \
  "s_branch RLRET_%=\n" \
  "FIN_%=:\n" \
  "s_setprio 0\n" \
  "s_cmp_eq_u32 s86, 0\n" \
  "s_cbranch_scc1 END_%=\n" \
  "s_mov_b32 s70, s69\n" \
  "s_cmp_lt_i32 s41, 0\n" \
  "s_cselect_b32 s69, 1, 2\n" \
  "s_cmp_lt_i32 s42, 1\n" \
  "s_cselect_b32 s69, 0, s69\n" \
  "s_add_u32 s41, s41, 1\n" \
  "s_sub_u32 s42, s42, 1\n" \
  "s_waitcnt vmcnt(0) lgkmcnt(0)\n" \
  "s_barrier\n" \
  "s_cmp_lt_u32 s69, 1\n" \
  "s_cbranch_scc1 DK0EN_%=\n" \
  "s_mov_b32 m0, s45\n" \
  "s_nop 0\n" \
  "buffer_load_dwordx4 v215, s[64:67], s43 offen sc1 lds\n" \
  "buffer_load_dwordx4 v215, s[64:67], s43 offen offset:1024 sc1 lds\n" \
  "s_add_u32 s43, s43, 0x8000\n" \
  "DK0EN_%=:\n" \
  "s_cmp_lt_u32 s69, 2\n" \
  "s_cbranch_scc1 DK1EN_%=\n" \
  "s_add_u32 m0, s45, 0x4000\n" \
  "s_nop 0\n" \
  "buffer_load_dwordx4 v215, s[64:67], s43 offen sc1 lds\n" \
  "buffer_load_dwordx4 v215, s[64:67], s43 offen offset:1024 sc1 lds\n" \
  "s_add_u32 s43, s43, 0x8000\n" \
  "DK1EN_%=:\n" \
  "s_cmp_lt_u32 s70, 1\n" \
  "s_cbranch_scc1 DV0EN_%=\n" \
  "s_mov_b32 m0, s46\n" \
  "s_nop 0\n" \
  "buffer_load_dwordx4 v215, s[64:67], s44 offen sc1 lds\n" \
  "buffer_load_dwordx4 v215, s[64:67], s44 offen offset:1024 sc1 lds\n" \
  "s_add_u32 s44, s44, 0x8000\n" \
  "DV0EN_%=:\n" \
  "s_cmp_lt_u32 s70, 2\n" \
  "s_cbranch_scc1 DV1EN_%=\n" \
  "s_add_u32 m0, s46, 0x4000\n" \
  "s_nop 0\n" \
  "buffer_load_dwordx4 v215, s[64:67], s44 offen sc1 lds\n" \
  "buffer_load_dwordx4 v215, s[64:67], s44 offen offset:1024 sc1 lds\n" \
  "s_add_u32 s44, s44, 0x8000\n" \
  "DV1EN_%=:\n" \
  "s_xor_b32 s45, s45, 0x8000\n" \
  "s_xor_b32 s46, s46, 0x8000\n" \
  "s_cmp_eq_i32 s42, -1\n" \
  "s_cbranch_scc0 RLRETN_%=\n" \
  "RLN_%=:\n" \
  "s_mov_b32 m0, s78\n" \
  "s_add_u32 s78, s78, 0x400\n" \
  "buffer_load_dwordx4 v219, s[72:75], s76 offen lds\n" \
  "s_add_u32 s76, s76, s77\n" \
  "s_mov_b32 m0, s78\n" \
  "s_add_u32 s78, s78, 0x400\n" \
  "buffer_load_dwordx4 v219, s[72:75], s76 offen lds\n" \
  "s_add_u32 s76, s76, s77\n" \
  "s_mov_b32 m0, s78\n" \
  "s_add_u32 s78, s78, 0x400\n" \
  "buffer_load_dwordx4 v219, s[72:75], s76 offen lds\n" \
  "s_add_u32 s76, s76, s77\n" \
  "s_mov_b32 m0, s78\n" \
  "s_add_u32 s78, s78, 0x400\n" \
  "buffer_load_dwordx4 v219, s[72:75], s76 offen lds\n" \
  "s_add_u32 s76, s76, s77\n" \
  "s_branch RLRETN_%=\n" \
  "RLRETN_%=:\n" \
  "END_%=:\n"
#define ATTN_CLOB "v64", "v65", "v66", "v67", "v68", "v69", "v70", "v71", "v72", "v73", "v74", "v75", "v76", "v77", "v78", "v79", "v80", "v81", "v82", "v83", "v84", "v85", "v86", "v87", "v88", "v89", "v90", "v91", "v92", "v93", "v94", "v95", "v144", "v145", "v146", "v147", "v148", "v149", "v150", "v151", "v152", "v153", "v154", "v155", "v156", "v157", "v158", "v159", "v160", "v161", "v162", "v163", "v164", "v165", "v166", "v167", "v168", "v169", "v170", "v171", "v172", "v173", "v174", "v175", "v176", "v177", "v178", "v179", "v180", "v181", "v182", "v183", "v184", "v185", "v186", "v187", "v188", "v189", "v190", "v191", "v192", "v193", "v194", "v195", "v196", "v197", "v198", "v199", "v205", "v206", "v207", "v208", "v209", "v210", "v211", "v212", "v213", "v216", "v217", "v218", "v220", "v221", "s52", "s53", "s55", "s56", "s57", "s58", "s59", "s60", "s61", "s62", "s63", "s68", "s70", "s71", "vcc", "scc", "memory"
#define AR_KA "v200"
#define AR_KB "v201"
#define AR_VA "v202"
#define AR_VB "v203"
#define AR_L "v204"
#define AR_DQ0 "v214"
#define AR_VL16 "v215"
#define AR_VRL "v219"
constexpr int K2_KS = 0, K2_VS = 65536, K2_QB = 131072, K2_LDS = 163840;
constexpr float QSCALE = 0.08838834764831845f * 1.4426950408889634f;
#define EXP2(x) __builtin_amdgcn_exp2f(x)

extern "C" __global__ __launch_bounds__(512, 2) void k2_attn(const __bf16* __restrict__ ckv, const __bf16* __restrict__ wuk, const __bf16* __restrict__ wuv, const __bf16* __restrict__ wq, const __bf16* __restrict__ wo, const __bf16* __restrict__ w1,
                                                  const __bf16* __restrict__ w2, char* __restrict__ ws) {
  extern __shared__ __attribute__((aligned(16))) char smem[];
  LAS char* lds = (LAS char*)smem;
  const unsigned ldsb = (unsigned)(size_t)lds;
  const int bid = blockIdx.x, tid = threadIdx.x, lane = tid & 63;
  const int wid = __builtin_amdgcn_readfirstlane(tid >> 6);
  const int idx = bid >> 3, h = (bid & 7) + 8 * (idx & 3), j = idx >> 2;
  const int grp = wid >> 2, rg = wid & 3, l31 = lane & 31, hh = lane >> 5;
  const int blkB = 15 - j, n1 = 2 * j + 2;
  rsrc_t sWq = mk_rsrc(wq, (size_t)DM * HDT * 2);
  rsrc_t sX = mk_rsrc(ws + WS_XNF, (size_t)SEQ * DM * 2), sKV = mk_rsrc(ws + WS_KVF, (size_t)SEQ * HDT * 4);
  rsrc_t sCtx = mk_rsrc(ws + WS_CTX, (size_t)128 * CST * 1024);
  const unsigned vl16 = lane * 16;
  const unsigned kvh = (unsigned)h * 32u * 32768u + wid * 2048;
  auto dma_tile = [&](int tile, int isv, unsigned lds_off) {
    const unsigned so = kvh + (unsigned)tile * 32768u + isv * 16384, la = ldsb + lds_off + wid * 2048;
    asm volatile("s_mov_b32 m0, %0\n\ts_nop 0\n\tbuffer_load_dwordx4 %1, %2, %3 offen sc1 lds\n\tbuffer_load_dwordx4 %1, %2, %3 offen offset:1024 sc1 lds"
                 ::"s"(la), "v"(vl16), "s"(sKV), "s"(so) : "memory");
  };
  bf16x8 q[8];
  unsigned* cntH = (unsigned*)(ws + WS_CNT + 8192) + h * 32;
  {
    const unsigned vW = (unsigned)((((lane >> 2) & 3) * HDT + (lane >> 4) * 32 + ((lane & 3) ^ (wid & 2)) * 8) * 2);
    constexpr unsigned coff[5] = {K2_QB, K2_KS, K2_VS, K2_KS + 32768, K2_VS + 32768};
    const unsigned wq0 = (unsigned)(wid * 4 * HDT + h * HD) * 2u, lw0 = ldsb + wid * 1024;
    auto issueW1 = [&](int c, int i) { dma16(sWq, vW, sop(wq0) + (c * 128 + 32 * i) * HDT * 2, sop(lw0) + coff[c] + i * 8192); };
    auto issueW = [&](int c) {
#pragma unroll
      for (int i = 0; i < 4; ++i) issueW1(c, i);
    };
    const unsigned xbase = (unsigned)(((grp ? blkB : j) * 4 + rg) * 40) * 1024u;
    bf16x8 xf[3][8];
    auto issueX = [&](bf16x8(&d)[8], int c) {
      const unsigned so = sop(xbase) + c * 4096;
      gld16<0>(d[0], sX, vl16, so); gld16<1024>(d[1], sX, vl16, so); gld16<2048>(d[2], sX, vl16, so); gld16<3072>(d[3], sX, vl16, so);
      gld16<0>(d[4], sX, vl16, so + 20480); gld16<1024>(d[5], sX, vl16, so + 20480); gld16<2048>(d[6], sX, vl16, so + 20480); gld16<3072>(d[7], sX, vl16, so + 20480);
    };
#define PINX(d) asm volatile("" : "+v"(d[0]), "+v"(d[1]), "+v"(d[2]), "+v"(d[3]), "+v"(d[4]), "+v"(d[5]), "+v"(d[6]), "+v"(d[7]))
    {
      rsrc_t sC = mk_rsrc(ckv, (size_t)SEQ * DC * 2), sWk = mk_rsrc(wuk, (size_t)DC * HDT * 2), sWv = mk_rsrc(wuv, (size_t)DC * HDT * 2);
      auto nx = [&](int w_) { if (w_ == 0) issueX(xf[0], 0); else { wait_vm<2>(); PINX(xf[0]); } };
      if (wid < 4) kv_body<false>(lds, sC, sWk, sWv, sKV, sWq, h, j, wid, lane, vW, issueW, nx);
      else kv_body<true>(lds, sC, sWk, sWv, sKV, sWq, h, j, wid, lane, vW, issueW, nx);
    }
    issueX(xf[1], 1);
    const unsigned wb0 = ldsb + (lane >> 4) * 2048 + ((lane >> 2) & 3) * 64 + ((lane >> 4) & 1) * 32 + (lane & 3) * 8;
    LAS const char* wb[2];
    f32x4 qa[8][2];
    constexpr int PQ = 8;
    bf16x8 wf[PQ + 1];
    static_for<-PQ, 160>([&](auto I_) {
      constexpr int i = decltype(I_)::value, r = i + PQ;
      if constexpr (r < 160) {
        if constexpr (r % 32 == 0) {
          constexpr int c = r / 32;
          if constexpr (c == 3) {
            if (wid == 0) {
              for (int it = 0; it < 400000; ++it) {
                if (__builtin_amdgcn_readfirstlane(__hip_atomic_load(cntH, __ATOMIC_RELAXED, __HIP_MEMORY_SCOPE_AGENT)) >= 8u) break;
                __builtin_amdgcn_s_sleep(1);
              }
            }
          }
          wait_bar<c == 0 ? 10 : c == 1 ? 0 : c < 4 ? 12 : 2>();
          PINX(xf[c % 3]);
          if constexpr (c == 1) { if (tid == 0) __hip_atomic_fetch_add(cntH, 1u, __ATOMIC_RELAXED, __HIP_MEMORY_SCOPE_AGENT); }
          if constexpr (c == 3) { dma_tile(0, 0, K2_KS); dma_tile(0, 1, K2_VS); }
          if constexpr (c == 4) dma_tile(1, 0, K2_KS + 32768);
          wb[0] = (LAS const char*)(size_t)(wb0 + coff[c]); opaque(wb[0]); wb[1] = (LAS const char*)(size_t)((wb0 ^ 32) + coff[c]); opaque(wb[1]);
        }
        if constexpr (PRIO_KV && r % 8 == 0) __builtin_amdgcn_s_setprio(3 - (r / 8) % 4);
        constexpr int k = (r % 32) / 8, d = r % 8;
        wf[r % (PQ + 1)] = lds_tr2(wb[d & 1] + k * 8192 + (d >> 1) * 256, wb[d & 1] + k * 8192 + (d >> 1) * 256 + 1024);
      }
      if constexpr (i >= 0) {
        constexpr int c = i / 32, k = (i % 32) / 8, d = i % 8;
        qa[d][0] = MFMA16(wf[i % (PQ + 1)], xf[c % 3][k], i < 8 ? f32x4{} : qa[d][0]);
        qa[d][1] = MFMA16(wf[i % (PQ + 1)], xf[c % 3][4 + k], i < 8 ? f32x4{} : qa[d][1]);
        if constexpr (c == 0 ? i < 12 : (c < 3 && i % 32 < 24 && (i & 1) == 0)) {
          constexpr int u = c == 0 ? i : (i % 32) / 2;
          if constexpr (u < 4) issueW1(c + 2, u);
          else gld16<((u - 4) & 3) * 1024>(xf[(c + 2) % 3][u - 4], sX, vl16, sop(xbase) + (c + 2) * 4096 + ((u - 4) >> 2) * 20480);
        }
      }
      SBAR();
    });
#pragma unroll
    for (int d = 0; d < 4; ++d) {
      u32x4 x0 = pack44(qa[2 * d][0], qa[2 * d + 1][0], QSCALE), x1 = pack44(qa[2 * d][1], qa[2 * d + 1][1], QSCALE);
      rows16_to_32(x0, x1);
      q[2 * d] = __builtin_bit_cast(bf16x8, x0); q[2 * d + 1] = __builtin_bit_cast(bf16x8, x1);
    }
    if (grp) {
#pragma unroll
      for (int k = 0; k < 8; ++k) *(LAS bf16x8*)(lds + K2_QB + rg * 8192 + k * 1024 + lane * 16) = q[k];
    }
  }
  f32x16 o[4], negm;
  float l = 0.f;
#pragma unroll
  for (int d = 0; d < 4; ++d) o[d] = f32x16{};
  negm = f32x16{};
  auto store_ctx = [&](const f32x16& oo, int d, float sc, int row0_) {
    const unsigned tg = (unsigned)(row0_ + l31) >> 4;
    const unsigned vo = (tg * CST + h * 4 + d) * 1024 + ((l31 & 15) + 16 * hh) * 16;
#pragma unroll
    for (int c = 0; c < 2; ++c) __builtin_amdgcn_raw_buffer_store_b128(pack8(oo, c, sc), sCtx, vo + c * 512, 0, ST_AUX);
  };
  int row0 = (grp ? blkB : j) * 128 + rg * 32;
  typedef unsigned u32x16 __attribute__((ext_vector_type(16)));
  u32x16 qlo, qhi;
#pragma unroll
  for (int k = 0; k < 4; ++k)
#pragma unroll
    for (int e = 0; e < 4; ++e) { qlo[4 * k + e] = __builtin_bit_cast(u32x4, q[k])[e]; qhi[4 * k + e] = __builtin_bit_cast(u32x4, q[4 + k])[e]; }
  int sKn = 2 - n1, sKrem = 15, sCK = 1;
  unsigned sKoff = kvh + 2 * 32768u, sVoff = kvh + 32768u + 16384u;
  unsigned sMK = ldsb + K2_KS + wid * 2048, sMV = ldsb + K2_VS + 32768 + wid * 2048;
  unsigned long long forceA = ~0ull; float thr = -3.0e38f;
  auto ukind = [&](int u) { return u < 1280 ? 0 : (u < 1820 ? 1 : 2); };
  auto uloc = [&](int u, int& ks, int& nb_) { const int k = ukind(u), c = u - (k == 0 ? 0 : (k == 1 ? 1280 : 1820)); ks = k == 1 ? c / 27 : c / 10; nb_ = c - ks * (k == 1 ? 27 : 10); };
  auto ufinish = [&](int u, LAS const char* img) {
    int ks, nb_; uloc(u, ks, nb_);
    const int k = ukind(u), nks = k == 0 ? CST : (k == 1 ? 20 : 54);
    relayout_finish(k ? 1 : 0, img, ws + (k == 0 ? WS_WOF : (k == 1 ? WS_W1F : WS_W2F)) + ((size_t)(nb_ * 4) * nks + ks) * 1024, (size_t)nks * 1024, lane,
                    k == 1 ? (float*)(ws + WS_CSP) + ks * FF + nb_ * 64 : nullptr);
  };
  const int uA = bid * 8 + wid, kA = ukind(uA), uld = kA == 1 ? FF : DM;
  int uks, unb; uloc(uA, uks, unb);
  rsrc_t sRL = mk_rsrc(kA == 0 ? wo : (kA == 1 ? w1 : w2), kA == 0 ? (size_t)HDT * DM * 2 : (size_t)DM * FF * 2);
  const unsigned vRL = (unsigned)(((lane >> 5) * 4 + ((lane >> 2) & 3)) * uld + ((lane >> 4) & 1) * 32 + (lane & 3) * 8) * 2u;
  unsigned rlOff = (unsigned)(uks * 32 * uld + unb * 64) * 2u, rlLds = ldsb + K2_KS + 32768 + wid * 4096;
  const unsigned rlStep = (unsigned)uld * 16u;
  int rlWarm = 12 - (int)((unsigned)(idx * 3 + (wid >> 2)) % 11u);
#pragma unroll 1
  for (int call = 0; call < 2; ++call) {
    const int nb = call ? n1 : 0;
    const unsigned sub = (call && grp == 0) ? 16384u : 0u;
    const int skip_last = (int)sop((unsigned)(wid - 2) >> 31);
    int cnt = (call ? 17 - n1 : n1) - skip_last, sKey = nb * 64 + (sub ? 64 : 0), sStep = call ? 128 : 64, sRowM = row0 - 64;
    unsigned aKA = ldsb + K2_KS + sub + vl16, aKB = aKA + 32768, aVB = ldsb + K2_VS + sub + vl16, aVA = aVB + 32768;
    int dq0 = row0 + l31 - 4 * hh;
    asm volatile(ATTN_ASM
                 : "+{v[0:15]}"(o[0]), "+{v[16:31]}"(o[1]), "+{v[32:47]}"(o[2]), "+{v[48:63]}"(o[3]), "+{v[96:111]}"(negm),
                   "+{" AR_KA "}"(aKA), "+{" AR_KB "}"(aKB), "+{" AR_VA "}"(aVA), "+{" AR_VB "}"(aVB), "+{" AR_L "}"(l),
                   "+{s40}"(cnt), "+{s41}"(sKn), "+{s42}"(sKrem), "+{s43}"(sKoff), "+{s44}"(sVoff), "+{s45}"(sMK), "+{s46}"(sMV), "+{s47}"(sKey),
                   "+{s[50:51]}"(forceA), "+{s54}"(thr), "+{s69}"(sCK), "+{s76}"(rlOff), "+{s78}"(rlLds), "+{s87}"(rlWarm)
                 : "{v[112:127]}"(qlo), "{v[128:143]}"(qhi), "{" AR_DQ0 "}"(dq0), "{" AR_VL16 "}"(vl16), "{s48}"(sStep), "{s49}"(sRowM), "{s[64:67]}"(sKV),
                   "{" AR_VRL "}"(vRL), "{s77}"(rlStep), "{s[72:75]}"(sRL), "{s86}"(skip_last)
                 : ATTN_CLOB);
    forceA = 0;
    if (call == 0) {
      if (grp == 0) {
        const int uE = 2048 + bid * 2 + wid;
        const bool early = wid < 2 && uE < 2360;
        if (early) { int ks, nb_; uloc(uE, ks, nb_); relayout_issue(mk_rsrc(w2, (size_t)DM * FF * 2), DM, ks * 32, nb_ * 64, ldsb + K2_VS + 49152 + wid * 4096, lane); }
        const float inv = __builtin_amdgcn_rcpf(swap32_sum(l));
#pragma unroll
        for (int k = 0; k < 4; ++k) {
          const u32x4 a = *(LAS const u32x4*)(lds + K2_QB + rg * 8192 + k * 1024 + lane * 16), b = *(LAS const u32x4*)(lds + K2_QB + rg * 8192 + (4 + k) * 1024 + lane * 16);
#pragma unroll
          for (int e = 0; e < 4; ++e) { qlo[4 * k + e] = a[e]; qhi[4 * k + e] = b[e]; }
        }
#pragma unroll
        for (int d = 0; d < 4; ++d) { store_ctx(o[d], d, inv, row0); o[d] = f32x16{}; }
        negm = f32x16{}; l = 0.f; row0 = blkB * 128 + rg * 32; forceA = ~0ull; thr = -3.0e38f;
        if (early) { wait_vm<8>(); ufinish(uE, lds + K2_VS + 49152 + wid * 4096); }
      }
    }
  }
  if (wid < 2) {
#pragma unroll 1
    for (int i = 0; i < 2; ++i) ufinish(bid * 8 + wid * 2 + i, lds + K2_KS + 32768 + (wid * 2 + i) * 4096);
  }
  u32x4 cw = {};
  if (wid == 0) {
    unsigned long long c0, c1;
    asm volatile("s_getpc_b64 s[20:21]\n\ts_add_u32 s20, s20, k3_tail@rel32@lo+4\n\ts_addc_u32 s21, s21, k3_tail@rel32@hi+12" : "={s[20:21]}"(c0)::"scc");
    asm volatile("s_getpc_b64 s[22:23]\n\ts_add_u32 s22, s22, k9_end@rel32@lo+4\n\ts_addc_u32 s23, s23, k9_end@rel32@hi+12" : "={s[22:23]}"(c1)::"scc");
    const unsigned long long o = (unsigned long long)idx * 1024 + lane * 16;
    if (c1 > c0 && o + 16 <= c1 - c0) cw = *(const __attribute__((address_space(1))) u32x4*)(c0 + o);
  }
  {
    const float lt = swap32_sum(l);
    const float m = lt > 0.f ? -negm[0] : -1e30f;
    LAS char* xa = lds + K2_KS;
    LAS char* xb = lds + K2_QB;
    LAS char* xs = lds + K2_VS + 32768;
    LAS char* mine = (grp ? xb : xa) + rg * 8192 + lane * 16;
    LAS const char* other = (grp ? xa : xb) + rg * 8192 + lane * 16;
    auto merge = [&](const f32x16& g0, const f32x16& g1, f32x16& k0, f32x16& k1, int dk) {
#pragma unroll
      for (int r = 0; r < 4; ++r) {
        *(LAS f32x4*)(mine + r * 1024) = (f32x4){g0[4 * r], g0[4 * r + 1], g0[4 * r + 2], g0[4 * r + 3]};
        *(LAS f32x4*)(mine + (4 + r) * 1024) = (f32x4){g1[4 * r], g1[4 * r + 1], g1[4 * r + 2], g1[4 * r + 3]};
      }
      *(LAS f32x2*)(xs + wid * 512 + lane * 8) = (f32x2){m, lt};
      if (dk == 0 && MERGE_RL_EARLY) ufinish(uA, lds + K2_KS + 32768 + wid * 4096);
      bar();
      const f32x2 ml = *(LAS const f32x2*)(xs + (wid ^ 4) * 512 + lane * 8);
      const float mn = fmaxf(m, ml[0]);
      const float a = EXP2(m - mn), b = EXP2(ml[0] - mn);
      const float inv = __builtin_amdgcn_rcpf(a * lt + b * ml[1]);
      const float fa = a * inv, fb = b * inv;
#pragma unroll
      for (int r = 0; r < 4; ++r) {
        const f32x4 t = *(LAS const f32x4*)(other + r * 1024), u = *(LAS const f32x4*)(other + (4 + r) * 1024);
#pragma unroll
        for (int e = 0; e < 4; ++e) { k0[4 * r + e] = k0[4 * r + e] * fa + t[e] * fb; k1[4 * r + e] = k1[4 * r + e] * fa + u[e] * fb; }
      }
      store_ctx(k0, dk, 1.0f, row0); store_ctx(k1, dk + 1, 1.0f, row0);
    };
    if (grp) merge(o[2], o[3], o[0], o[1], 0); else merge(o[0], o[1], o[2], o[3], 2);
  }
  if (grp && !MERGE_RL_EARLY) ufinish(uA, lds + K2_KS + 32768 + wid * 4096);
  asm volatile("" ::"v"(cw));
}

constexpr int T_RED = 86016, T_CS = 147456, T_B1 = T_CS + 512, T_LDS = T_B1 + 1024;
DI void gld8(u32x2& dst, rsrc_t srd, unsigned voff, unsigned soff) {
  asm volatile("buffer_load_dwordx2 %0, %1, %2, %3 offen" : "=v"(dst) : "v"(voff), "s"(srd), "s"(soff) : "memory");
}
DI void gld8c(f32x2& dst, rsrc_t srd, unsigned voff, unsigned soff) {
  asm volatile("buffer_load_dwordx2 %0, %1, %2, %3 offen sc1" : "=v"(dst) : "v"(voff), "s"(srd), "s"(soff) : "memory");
}
template <int IMM> DI void gld16c(bf16x8& dst, rsrc_t srd, unsigned voff, unsigned soff) {
  asm volatile("buffer_load_dwordx4 %0, %1, %2, %3 offen offset:%4 sc1" : "=v"(dst) : "v"(voff), "s"(srd), "s"(soff), "i"(IMM) : "memory");
}
template <int IMM> DI void gld16a(bf16x8& dst, rsrc_t srd, unsigned voff, unsigned soff) {
  asm volatile("buffer_load_dwordx4 %0, %1, %2, %3 offen offset:%4" : "=a"(dst) : "v"(voff), "s"(srd), "s"(soff), "i"(IMM) : "memory");
}
template <bool ACC> DI void pin5(bf16x8 (&f)[5]) {
  if constexpr (ACC) asm volatile("" : "+a"(f[0]), "+a"(f[1]), "+a"(f[2]), "+a"(f[3]), "+a"(f[4]));
  else asm volatile("" : "+v"(f[0]), "+v"(f[1]), "+v"(f[2]), "+v"(f[3]), "+v"(f[4]));
}
DI void st16c(void* p, u32x4 v) { asm volatile("global_store_dwordx4 %0, %1, off sc1\n\ts_nop 1" ::"v"(p), "v"(v) : "memory"); }
DI void st8c(void* p, f32x2 v) { asm volatile("global_store_dwordx2 %0, %1, off sc1" ::"v"(p), "v"(v) : "memory"); }
DI void panel_signal(unsigned* cnt) {
  asm volatile("s_waitcnt vmcnt(0) lgkmcnt(0)\n\ts_barrier" ::: "memory");
  if (threadIdx.x == 0) __hip_atomic_fetch_add(cnt, 1u, __ATOMIC_RELAXED, __HIP_MEMORY_SCOPE_AGENT);
}
DI void panel_wait(unsigned* cnt, int wid) {
  if (wid == 0) {
    for (int it = 0; it < 400000; ++it) {
      if (__builtin_amdgcn_readfirstlane(__hip_atomic_load(cnt, __ATOMIC_RELAXED, __HIP_MEMORY_SCOPE_AGENT)) >= 16u) break;
      __builtin_amdgcn_s_sleep(1);
    }
  }
  asm volatile("s_waitcnt vmcnt(0) lgkmcnt(0)\n\ts_barrier" ::: "memory");
}

extern "C" __global__ __launch_bounds__(256, 1) void k3_tail(const __bf16* __restrict__ x, const __bf16* __restrict__ b1, const __bf16* __restrict__ b2,
                                                  __bf16* __restrict__ out, char* __restrict__ ws) {
  extern __shared__ __attribute__((aligned(16))) char smem[];
  LAS char* lds = (LAS char*)smem;
  const unsigned ldsb = (unsigned)(size_t)lds;
  const int bid = blockIdx.x, tid = threadIdx.x, lane = tid & 63;
  const int wid = __builtin_amdgcn_readfirstlane(tid >> 6);
  const int kq = bid & 3, pn = ((bid >> 2) & 1) * 8 + (bid >> 5), n4 = (bid >> 3) & 3, w16 = kq * 4 + n4;
  const int mb = pn * 2 + (kq >> 1), nb = n4 * 2 + (kq & 1);
  const int ng0 = (w16 >> 2) * 27 + (w16 & 3) * 7, nng = (w16 & 3) == 3 ? 6 : 7;
  unsigned* cntA = (unsigned*)(ws + WS_CNT) + pn * 64; unsigned* cntB = cntA + 32;
  const unsigned vl16 = lane * 16;
  const int tg = mb * 4 + wid, tok = tg * 16 + (lane & 15), g = lane >> 4;
  constexpr int PD = T_PD;
  rsrc_t sW1 = mk_rsrc(ws + WS_W1F, (size_t)DM * FF * 2);
  auto issueW1 = [&](int c) {
    const unsigned so = sop((unsigned)(ng0 * 20 + wid) * 1024u), la = sop(ldsb + wid * 1024);
#pragma unroll
    for (int n = 0; n < 7; ++n) dma16(sW1, vl16, so + (n * 20 + 4 * c) * 1024, la + (c * 7 + n) * 4096);
  };
  auto reduce = [&](f32x4 (&acc)[5][4], f32x4 (&r)[5]) {
#pragma unroll
    for (int q = 1; q < 4; ++q) {
      LAS char* wp = (LAS char*)(size_t)(vl16 + sop(ldsb + T_RED + ((wid + q) & 3) * 15360 + (q - 1) * 5120)); opaque(wp);
#pragma unroll
      for (int n = 0; n < 5; ++n) *(LAS f32x4*)(wp + n * 1024) = acc[n][q];
    }
    LAS const char* rp = (LAS const char*)(size_t)(vl16 + sop(ldsb + T_RED + wid * 15360)); opaque(rp);
    bar();
#pragma unroll
    for (int n = 0; n < 5; ++n) r[n] = acc[n][0];
#pragma unroll
    for (int q = 0; q < 3; ++q)
#pragma unroll
      for (int n = 0; n < 5; ++n) r[n] += *(LAS const f32x4*)(rp + (q * 5 + n) * 1024);
  };
  u32x2 hw[5];
  {
    rsrc_t sA = mk_rsrc(ws + WS_CTX, (size_t)128 * CST * 1024), sB = mk_rsrc(ws + WS_WOF, (size_t)40 * CST * 1024);
    unsigned sa[4], sb[5];
#pragma unroll
    for (int q = 0; q < 4; ++q) sa[q] = (unsigned)((mb * 4 + ((q + wid) & 3)) * CST + 32 * wid) * 1024u;
#pragma unroll
    for (int n = 0; n < 5; ++n) sb[n] = (unsigned)((nb * 5 + n) * CST + 32 * wid) * 1024u;
    f32x4 acc[5][4];
    bf16x8 fa[PD][4], fb[PD][5];
    u32x2 e0[5];
    float csv[10];
    static_for<-PD, 32>([&](auto T_) {
      constexpr int t = decltype(T_)::value, r = t + PD, bi = (t + PD) % PD;
      if constexpr (t >= 0) {
        constexpr int younger = (31 - t < PD - 1 ? 31 - t : PD - 1) * 9 + (t > T_R0 - PD && t <= T_R0 ? 16 : 0) + (t > 33 - PD ? 7 : 0) + (t > 34 - PD ? 7 : 0);
        asm volatile("s_waitcnt vmcnt(%9)" : "+v"(fa[bi][0]), "+v"(fa[bi][1]), "+v"(fa[bi][2]), "+v"(fa[bi][3]), "+v"(fb[bi][0]), "+v"(fb[bi][1]),
                     "+v"(fb[bi][2]), "+v"(fb[bi][3]), "+v"(fb[bi][4]) : "i"(younger) : "memory");
      }
      if constexpr (r < 32 && (r & 3) == 0 && r > 0) {
#pragma unroll
        for (int q = 0; q < 4; ++q) sa[q] = sop(sa[q] + 4096);
#pragma unroll
        for (int n = 0; n < 5; ++n) sb[n] = sop(sb[n] + 4096);
      }
#pragma unroll
      for (int n = 0; n < 5; ++n) {
        if constexpr (t >= 0) {
#pragma unroll
          for (int q = 0; q < 4; ++q) {
            acc[n][q] = MFMA16(fb[bi][n], fa[bi][q], t == 0 ? f32x4{} : acc[n][q]);
            if constexpr (r < 32) { if (n == 4) { SBAR(); gld16<(r & 3) * 1024>(fa[bi][q], sA, vl16, sa[q]); SBAR(); } }
          }
          SBAR();
        } else if (n == 4) {
#pragma unroll
          for (int q = 0; q < 4; ++q) gld16<(r & 3) * 1024>(fa[bi][q], sA, vl16, sa[q]);
        }
        if constexpr (r < 32) { gld16<(r & 3) * 1024>(fb[bi][n], sB, vl16, sb[n]); SBAR(); }
      }
      if constexpr (r == T_R0) {
        const unsigned xo = sop((unsigned)nb * 160u), po = sop((unsigned)((wid >> 1) * 10 * FF + ng0 * 16) * 4u);
#pragma unroll
        for (int n = 0; n < 5; ++n) gld8(e0[n], mk_rsrc(x, (size_t)SEQ * DM * 2), (unsigned)(tok * DM + 4 * g) * 2u, xo + n * 32);
        rsrc_t sP = mk_rsrc(ws + WS_CSP, (size_t)20 * FF * 4);
#pragma unroll
        for (int k = 0; k < 10; ++k)
          asm volatile("buffer_load_dword %0, %1, %2, %3 offen" : "=v"(csv[k]) : "v"((unsigned)(tid & 127) * 4u), "s"(sP), "s"(po + k * FF * 4) : "memory");
        dma16(mk_rsrc(b1, (size_t)FF * 2), vl16, (unsigned)ng0 * 32u, ldsb + T_B1);
      }
      if constexpr (r == 33) issueW1(0);
      if constexpr (r == 34) issueW1(1);
      SBAR();
    });
    asm volatile("" : "+v"(e0[0]), "+v"(e0[1]), "+v"(e0[2]), "+v"(e0[3]), "+v"(e0[4]), "+v"(csv[0]), "+v"(csv[1]), "+v"(csv[2]), "+v"(csv[3]),
                 "+v"(csv[4]), "+v"(csv[5]), "+v"(csv[6]), "+v"(csv[7]), "+v"(csv[8]), "+v"(csv[9])::"memory");
    const float cs = (((csv[0] + csv[1]) + (csv[2] + csv[3])) + ((csv[4] + csv[5]) + (csv[6] + csv[7]))) + (csv[8] + csv[9]);
    if (wid >= 2) *(LAS float*)(lds + T_CS + (tid & 127) * 4) = cs;
    f32x4 r[5];
    reduce(acc, r);
    if (wid < 2) *(LAS float*)(lds + T_CS + tid * 4) += cs;
#pragma unroll
    for (int n = 0; n < 5; ++n) {
      hw[n][0] = cvtpk(r[n][0] + bf_lo(e0[n][0]), r[n][1] + bf_hi(e0[n][0]));
      hw[n][1] = cvtpk(r[n][2] + bf_lo(e0[n][1]), r[n][3] + bf_hi(e0[n][1]));
    }
#pragma unroll
    for (int n = 0; n < 6; n += 2) {
      const u32x4 v = pair_rows(hw[n], hw[n == 4 ? 4 : n + 1]);
      const int ng = nb * 5 + (n == 4 ? 4 : n + (g & 1));
      if (n < 4 || (g & 1) == 0) st16c(ws + WS_HF + (size_t)(tg * 40 + ng) * 512 + (g >> 1) * 256 + (lane & 15) * 16, v);
    }
    float v[20]; float s = 0.f;
#pragma unroll
    for (int n = 0; n < 5; ++n) {
      v[4 * n] = bf_lo(hw[n][0]); v[4 * n + 1] = bf_hi(hw[n][0]); v[4 * n + 2] = bf_lo(hw[n][1]); v[4 * n + 3] = bf_hi(hw[n][1]);
      s += (v[4 * n] + v[4 * n + 1]) + (v[4 * n + 2] + v[4 * n + 3]);
    }
    s = quad16_sum(s);
    float mean = s * (1.0f / 80.0f);
    opaque(mean);
    float q = 0.f, q2 = 0.f;
#pragma unroll
    for (int i = 0; i < 20; i += 2) { const float d = v[i] - mean, e = v[i + 1] - mean; q += d * d; q2 += e * e; }
    q += q2;
    q = quad16_sum(q);
    if (g == 0) st8c(ws + WS_ST + ((size_t)(tg * 8 + nb) * 16 + (lane & 15)) * 8, (f32x2){mean, q});
  }
  panel_signal(cntA);
  if (wid) {
    const unsigned so = sop((unsigned)(ng0 * 20 + 4 + 4 * wid) * 1024u), la = sop(ldsb + (1 + wid) * 28672);
#pragma unroll
    for (int n = 0; n < 7; ++n) dma16x4(sW1, vl16, so + n * 20480, la + n * 4096);
  }
  panel_wait(cntA, wid);
  bf16x8 w2f[14][5];
  const int ks0 = wid < 2 ? 14 * wid : 2 + 13 * wid;
  const unsigned oob = wid >= 2 ? 0x40000000u : 0u;
  rsrc_t sW2 = mk_rsrc(ws + WS_W2F, (size_t)40 * 54 * 1024);
  auto issueW2 = [&](auto R_) {
    constexpr int r = decltype(R_)::value;
    const unsigned b0 = sop((unsigned)(nb * 5 * 54 + ks0) * 1024u + (r == 13 ? oob : 0u));
#pragma unroll
    for (int n = 0; n < 5; ++n) {
      if constexpr (r < 8) gld16a<(r & 3) * 1024>(w2f[r][n], sW2, vl16, b0 + (n * 54 + (r & ~3)) * 1024);
      else gld16<(r & 3) * 1024>(w2f[r][n], sW2, vl16, b0 + (n * 54 + (r & ~3)) * 1024);
    }
  };
  {
    rsrc_t sH = mk_rsrc(ws + WS_HF, (size_t)SEQ * DM * 2), sS = mk_rsrc(ws + WS_ST, (size_t)SEQ * 64);
    const int tg0 = pn * 8 + wid * 2;
    bf16x8 hf[2][20];
    f32x2 sv[2][2];
    auto issueH = [&](auto C_) {
      constexpr int c = decltype(C_)::value;
#pragma unroll
      for (int t = 0; t < 2; ++t) {
        const unsigned so = sop((unsigned)tg0 * 20480u) + (t * 20 + 4 * c) * 1024;
        gld16c<0>(hf[t][4 * c], sH, vl16, so); gld16c<1024>(hf[t][4 * c + 1], sH, vl16, so);
        gld16c<2048>(hf[t][4 * c + 2], sH, vl16, so); gld16c<3072>(hf[t][4 * c + 3], sH, vl16, so);
      }
    };
    issueH(std::integral_constant<int, 0>{}); issueH(std::integral_constant<int, 1>{});
#pragma unroll
    for (int t = 0; t < 2; ++t)
#pragma unroll
      for (int e = 0; e < 2; ++e) gld8c(sv[t][e], sS, (unsigned)((2 * g + e) * 128 + (lane & 15) * 8), (unsigned)(tg0 + t) * 1024u);
    f32x4 acc[2][7];
    constexpr int PW = 4;
    bf16x8 wf[PW + 1];
    LAS const char* wb = lds + lane * 16;
    LAS const char* wb2 = wb + 57344; opaque(wb2);
    LAS const char* wb3 = wb + 114688; opaque(wb3);
    float rstd[2], mr[2];
    auto stats = [&](int t) {
      const float mean = quad16_sum(sv[t][0][0] + sv[t][1][0]) * 0.125f;
      const float d0 = sv[t][0][0] - mean, d1 = sv[t][1][0] - mean;
      const float m2 = quad16_sum(sv[t][0][1] + sv[t][1][1] + 80.0f * (d0 * d0 + d1 * d1));
      rstd[t] = __builtin_amdgcn_rsqf(m2 * (1.0f / DM) + 1e-5f); mr[t] = -mean * rstd[t];
    };
    u32x2 w[2][7]; f32x4 cs, bb, yv, zv;
    LAS const char* csp = lds + T_CS + g * 16; opaque(csp);
    LAS const char* b1p = lds + T_B1 + g * 8; opaque(b1p);
    char* mid = ws + WS_MID + (size_t)(tg0 * 108 + ng0) * 512 + (g >> 1) * 256 + (lane & 15) * 16;
    auto epi = [&](auto P_) {
      constexpr int pc = decltype(P_)::value, n = pc / 10, t = (pc / 5) & 1, e = pc % 5;
      if constexpr (e == 0) {
        if constexpr (t == 0) {
          cs = *(LAS const f32x4*)(csp + n * 64);
          const u32x2 bv = *(LAS const u32x2*)(b1p + n * 32);
          bb = (f32x4){bf_lo(bv[0]), bf_hi(bv[0]), bf_lo(bv[1]), bf_hi(bv[1])};
        }
        yv = acc[t][n] * rstd[t] + (cs * mr[t] + bb); opaque(yv);
      } else if constexpr (e == 1) { zv = yv * (yv * yv * -0.1029432397f + -2.3022081986f); opaque(zv); }
      else if constexpr (e == 2) {
#pragma unroll
        for (int q = 0; q < 4; ++q) zv[q] = __builtin_amdgcn_exp2f(zv[q]);
        opaque(zv);
      } else if constexpr (e == 3) {
        zv = zv + 1.0f;
#pragma unroll
        for (int q = 0; q < 4; ++q) zv[q] = __builtin_amdgcn_rcpf(zv[q]);
        opaque(zv);
      } else {
        yv = yv * zv;
        w[t][n][0] = cvtpk(yv[0], yv[1]); w[t][n][1] = cvtpk(yv[2], yv[3]);
        if constexpr (n & 1) st16c(mid + t * 108 * 512 + (n - 1 + (g & 1)) * 512, pair_rows(w[t][n - 1], w[t][n]));
        else if constexpr (n == 6) { const u32x4 v6 = pair_rows(w[t][6], w[t][6]); if (nng == 7 && (g & 1) == 0) st16c(mid + t * 108 * 512 + 6 * 512, v6); }
        else opaque(w[t][n]);
      }
    };
    static_for<-PW, 140>([&](auto I_) {
      constexpr int i = decltype(I_)::value, r = i + PW;
      if constexpr (r < 140) {
        constexpr int k = r < 60 ? r / 3 : (r - 60) % 20, n = r < 60 ? r % 3 : 3 + (r - 60) / 20, c = k / 4;
        constexpr int off = (c & 1) * 28672 + n * 4096 + (k & 3) * 1024;
        wf[r % (PW + 1)] = lds_rd16((c < 2 ? wb : (c < 4 ? wb2 : wb3)) + off);
      }
      if constexpr (i >= 0) {
        constexpr int k = i < 60 ? i / 3 : (i - 60) % 20, n = i < 60 ? i % 3 : 3 + (i - 60) / 20;
        if constexpr (i < 60 && i % 12 == 0) {
          constexpr int c = i / 12;
          constexpr int younger = c < 2 ? 12 : c < 4 ? 8 : 0;
          asm volatile("s_waitcnt vmcnt(%8)" : "+v"(hf[0][4 * c]), "+v"(hf[0][4 * c + 1]), "+v"(hf[0][4 * c + 2]), "+v"(hf[0][4 * c + 3]),
                       "+v"(hf[1][4 * c]), "+v"(hf[1][4 * c + 1]), "+v"(hf[1][4 * c + 2]), "+v"(hf[1][4 * c + 3]) : "i"(younger) : "memory");
          if constexpr (c < 3) issueH(std::integral_constant<int, c + 2>{});
          if constexpr (c == 3) { asm volatile("" : "+v"(sv[0][0]), "+v"(sv[0][1]), "+v"(sv[1][0]), "+v"(sv[1][1])); stats(0); stats(1); }
        }
        acc[0][n] = MFMA16(wf[i % (PW + 1)], hf[0][k], k == 0 ? f32x4{} : acc[0][n]);
        acc[1][n] = MFMA16(wf[i % (PW + 1)], hf[1][k], k == 0 ? f32x4{} : acc[1][n]);
        constexpr int u = i - 61;
        if constexpr (u >= 0 && u < 50) epi(std::integral_constant<int, u>{});
        if constexpr (u >= 60 && u < 70) epi(std::integral_constant<int, u - 10>{});
        if constexpr (i >= 52 && (i - 52) % 11 == 0 && (i - 52) / 11 < T_W2EARLY) issueW2(std::integral_constant<int, (i - 52) / 11>{});
      }
      SBAR();
    });
    static_for<60, 70>([&](auto P_) { epi(P_); });
  }
  panel_signal(cntB);
  u32x2 e1[5];
  static_for<T_W2EARLY, 14>([&](auto R_) { issueW2(R_); });
  {
    const unsigned bo = sop((unsigned)nb * 160u);
#pragma unroll
    for (int n = 0; n < 5; ++n) gld8(e1[n], mk_rsrc(b2, (size_t)DM * 2), (unsigned)g * 8u, bo + n * 32);
  }
  panel_wait(cntB, wid);
  {
    static_for<0, 14>([&](auto R_) {
      constexpr int r = decltype(R_)::value;
      pin5<(r < 8)>(w2f[r]);
    });
    asm volatile("" : "+v"(e1[0]), "+v"(e1[1]), "+v"(e1[2]), "+v"(e1[3]), "+v"(e1[4]));
    rsrc_t sA = mk_rsrc(ws + WS_MID, (size_t)128 * 54 * 1024);
    unsigned sa[4];
#pragma unroll
    for (int q = 0; q < 4; ++q) sa[q] = sop((unsigned)((mb * 4 + ((q + wid) & 3)) * 54 + ks0) * 1024u);
    constexpr int PC = 5;
    f32x4 acc[5][4];
    bf16x8 fa[PC][4];
    static_for<-PC, 14>([&](auto T_) {
      constexpr int t = decltype(T_)::value, r = t + PC, bi = (t + PC) % PC;
      if constexpr (t >= 0) {
        constexpr int younger = (13 - t < PC - 1 ? 13 - t : PC - 1) * 4;
        asm volatile("s_waitcnt vmcnt(%4)" : "+v"(fa[bi][0]), "+v"(fa[bi][1]), "+v"(fa[bi][2]), "+v"(fa[bi][3]) : "i"(younger) : "memory");
#pragma unroll
        for (int n = 0; n < 5; ++n)
#pragma unroll
          for (int q = 0; q < 4; ++q) acc[n][q] = MFMA16(w2f[t][n], fa[bi][q], t == 0 ? f32x4{} : acc[n][q]);
        SBAR();
      }
      if constexpr (r < 14) {
        if constexpr ((r & 3) == 0 && r > 0) {
#pragma unroll
          for (int q = 0; q < 4; ++q) sa[q] = sop(sa[q] + 4096);
        }
#pragma unroll
        for (int q = 0; q < 4; ++q) gld16c<(r & 3) * 1024>(fa[bi][q], sA, vl16, sa[q] + (r == 13 ? oob : 0u));
      }
      SBAR();
    });
    f32x4 r[5];
    reduce(acc, r);
    u32x2 w[5];
#pragma unroll
    for (int n = 0; n < 5; ++n) {
      w[n][0] = cvtpk(r[n][0] + bf_lo(e1[n][0]) + bf_lo(hw[n][0]), r[n][1] + bf_hi(e1[n][0]) + bf_hi(hw[n][0]));
      w[n][1] = cvtpk(r[n][2] + bf_lo(e1[n][1]) + bf_lo(hw[n][1]), r[n][3] + bf_hi(e1[n][1]) + bf_hi(hw[n][1]));
    }
#pragma unroll
    for (int n = 0; n < 6; n += 2) {
      const u32x4 v = pair_rows(w[n], w[n == 4 ? 4 : n + 1]);
      const int ng = nb * 5 + (n == 4 ? 4 : n + (g & 1));
      if (n < 4 || (g & 1) == 0) *(u32x4*)(out + (size_t)tok * DM + ng * 16 + (g >> 1) * 8) = v;
    }
  }
}

extern "C" __global__ void k9_end() {}

extern "C" void kernel_launch(void* const* d_in, const int* in_sizes, int n_in, void* d_out, int out_size, void* d_ws, size_t ws_size,
                              hipStream_t stream) {
  const __bf16* x = (const __bf16*)d_in[0];
  const __bf16* ckv = (const __bf16*)d_in[1];
  const __bf16* wq = (const __bf16*)d_in[2];
  const __bf16* wuk = (const __bf16*)d_in[3];
  const __bf16* wuv = (const __bf16*)d_in[4];
  const __bf16* wo = (const __bf16*)d_in[5];
  const __bf16* w1 = (const __bf16*)d_in[7];
  const __bf16* b1 = (const __bf16*)d_in[8];
  const __bf16* w2 = (const __bf16*)d_in[9];
  const __bf16* b2 = (const __bf16*)d_in[10];
  char* ws = (char*)d_ws;
  (void)hipFuncSetAttribute((const void*)k2_attn, hipFuncAttributeMaxDynamicSharedMemorySize, K2_LDS);
  (void)hipFuncSetAttribute((const void*)k3_tail, hipFuncAttributeMaxDynamicSharedMemorySize, T_LDS);
  k0_prep<<<256, 384, 0, stream>>>(x, ckv, wuk, wuv, b1, b2, ws);
  k2_attn<<<256, 512, K2_LDS, stream>>>(ckv, wuk, wuv, wq, wo, w1, w2, ws);
  k3_tail<<<256, 256, T_LDS, stream>>>(x, b1, b2, (__bf16*)d_out, ws);
}
```
